```python
import torch

def _softmax(x, axis=-1):
    return torch.softmax(x, dim=axis)
def _gelu(x):
    return torch.nn.functional.gelu(x, approximate="tanh")
def _silu(x):
    return torch.nn.functional.silu(x)
def _sigmoid(x):
    return torch.sigmoid(x)
def _layernorm(x, axis=-1, eps=1e-5):
    m = x.mean(dim=axis, keepdim=True)
    v = x.var(dim=axis, keepdim=True, unbiased=False)
    return (x - m) / torch.sqrt(v + eps)
def _scatter_add(tgt, idx, src, axis=0):
    return tgt.clone().index_add_(axis, idx, src)
def _segment_sum(src, seg, n):
    out = torch.zeros((n,) + src.shape[1:], dtype=src.dtype)
    return out.index_add_(0, seg, src)
def _pad(x, pw):
    flat = tuple(v for pair in reversed(pw) for v in pair)
    return torch.nn.functional.pad(x, flat)

kernel_name = 'concat_heads__mha_block_cached__gqa_block_cached__near_ridge'

def setup_inputs():

    _w = (lambda a: (lambda t: t.to(torch.bfloat16) if t.is_floating_point() else t)(torch.as_tensor(a)))
    def _A_setup():
        B, Sq, D = 8, 128, 448
        H, Sk, Dh = 16, 512, 256
        return {
            "x": _w((lambda *s: torch.randn(*s, dtype=torch.bfloat16) * 0.8)(B, Sq, D)),
            "k_cache": _w((lambda *s: torch.randn(*s, dtype=torch.bfloat16) * 0.8)(B, H, Sk, Dh)),
            "v_cache": _w((lambda *s: torch.randn(*s, dtype=torch.bfloat16) * 0.05)(B, H, Sk, Dh)),
            "wq": _w((lambda *s: torch.randn(*s, dtype=torch.bfloat16) * 0.8)(D, H * Dh) * (D ** -0.5)),
            "wo": _w((lambda *s: torch.randn(*s, dtype=torch.bfloat16) * 0.05)(H * Dh, D) * ((H * Dh) ** -0.5)),
        }
    _A_in = _A_setup()

    def _B_setup():
        B, Sq, D = 8, 128, 448
        Hq, Hkv, Sk, Dh = 16, 16, 512, 256
        assert Hq % Hkv == 0, (Hq, Hkv)
        return {
            "x": _w((lambda *s: torch.randn(*s, dtype=torch.bfloat16) * 0.8)(B, Sq, D)),
            "k_cache": _w((lambda *s: torch.randn(*s, dtype=torch.bfloat16) * 0.8)(B, Hkv, Sk, Dh)),
            "v_cache": _w((lambda *s: torch.randn(*s, dtype=torch.bfloat16) * 0.05)(B, Hkv, Sk, Dh)),
            "wq": _w((lambda *s: torch.randn(*s, dtype=torch.bfloat16) * 0.8)(D, Hq * Dh) * (D ** -0.5)),
            "wo": _w((lambda *s: torch.randn(*s, dtype=torch.bfloat16) * 0.05)(Hq * Dh, D) * ((Hq * Dh) ** -0.5)),
        }
    _B_in = _B_setup()

    return {
        "A_x": _A_in["x"],
        "A_k_cache": _A_in["k_cache"],
        "A_v_cache": _A_in["v_cache"],
        "A_wq": _A_in["wq"],
        "A_wo": _A_in["wo"],
        "B_x": _B_in["x"],
        "B_k_cache": _B_in["k_cache"],
        "B_v_cache": _B_in["v_cache"],
        "B_wq": _B_in["wq"],
        "B_wo": _B_in["wo"],
    }

def reference(A_x, A_k_cache, A_v_cache, A_wq, A_wo, B_x, B_k_cache, B_v_cache, B_wq, B_wo):
    def _A_ref(x, k_cache, v_cache, wq, wo):
        B, Sq, D = x.shape
        H, Dh = 16, 256
        q = (lambda x, *d: x.permute(*d))((x @ wq).reshape(B, Sq, H, Dh), 0, 2, 1, 3)
        scores = torch.einsum("bhqd,bhkd->bhqk", q, k_cache) * (Dh ** -0.5)
        p = _softmax(scores, axis=-1)
        o = torch.einsum("bhqk,bhkd->bhqd", p, v_cache)
        return (lambda x, *d: x.permute(*d))(o, 0, 2, 1, 3).reshape(B, Sq, H * Dh) @ wo

    def _B_ref(x, k_cache, v_cache, wq, wo):
        B, Sq, D = x.shape
        Hq, Dh = 16, 256
        Hkv = k_cache.shape[1]
        rep = Hq // Hkv
        q = (lambda x, *d: x.permute(*d))((x @ wq).reshape(B, Sq, Hq, Dh), 0, 2, 1, 3)
        k = (lambda x, n, axis: torch.repeat_interleave(x, n, dim=axis))(k_cache, rep, axis=1)
        v = (lambda x, n, axis: torch.repeat_interleave(x, n, dim=axis))(v_cache, rep, axis=1)
        scores = torch.einsum("bhqd,bhkd->bhqk", q, k) * (Dh ** -0.5)
        p = _softmax(scores, axis=-1)
        o = torch.einsum("bhqk,bhkd->bhqd", p, v)
        return (lambda x, *d: x.permute(*d))(o, 0, 2, 1, 3).reshape(B, Sq, Hq * Dh) @ wo

    a = _A_ref(A_x, A_k_cache, A_v_cache, A_wq, A_wo)
    b = _B_ref(B_x, B_k_cache, B_v_cache, B_wq, B_wo)
    return torch.concatenate([a, b], axis=-1)
```

```cpp
#include <hip/hip_runtime.h>
#include <hip/hip_bf16.h>
#include <stdint.h>

typedef short    bf16x8 __attribute__((ext_vector_type(8)));
typedef short    s16x4  __attribute__((ext_vector_type(4)));
typedef float    f32x16 __attribute__((ext_vector_type(16)));
typedef float    f32x2  __attribute__((ext_vector_type(2)));
typedef __bf16   bf16x2 __attribute__((ext_vector_type(2)));
typedef unsigned u32x4  __attribute__((ext_vector_type(4)));
#define LAS __attribute__((address_space(3)))
typedef LAS char* lds_ptr;

constexpr int NB = 8, SQ = 128, DM = 448, NH = 16, SK = 512, DH = 256;
constexpr int SLOT = 32768;
constexpr int LDS_BYTES = 5 * SLOT;
constexpr int PART_BYTES = SQ * DM * 2;
constexpr float SCL = 0.0625f * 1.4426950408889634f;

#define DI __device__ __forceinline__
#define MFMA(a, b, c) __builtin_amdgcn_mfma_f32_32x32x16_bf16((a), (b), (c), 0, 0, 0)
#define WAIT_BAR(N) asm volatile("s_waitcnt vmcnt(" #N ") lgkmcnt(0)\n\ts_barrier" ::: "memory")
#define LDS_BAR()   asm volatile("s_waitcnt lgkmcnt(0)\n\ts_barrier" ::: "memory")

DI void dma(__amdgpu_buffer_rsrc_t rs, unsigned voff, unsigned soff, unsigned ldsb) {
  asm volatile("s_mov_b32 m0, %0\n\ts_nop 0\n\tbuffer_load_dwordx4 %1, %2, %3 offen lds"
               :: "s"(ldsb), "v"(voff), "s"(rs), "s"(soff) : "memory");
}
DI unsigned pk(float a, float b) {
  const bf16x2 v = __builtin_convertvector((f32x2){a, b}, bf16x2);
  return __builtin_bit_cast(unsigned, v);
}
DI u32x4 pack8(const f32x16& x, int s) {
  return (u32x4){pk(x[8 * s], x[8 * s + 1]), pk(x[8 * s + 2], x[8 * s + 3]), pk(x[8 * s + 4], x[8 * s + 5]), pk(x[8 * s + 6], x[8 * s + 7])};
}
DI bf16x8 lds_b128(lds_ptr p) { return *(const LAS bf16x8*)p; }
DI void lds_w128(lds_ptr p, u32x4 v) { *(LAS u32x4*)p = v; }
DI s16x4 lds_tr(lds_ptr p) { return __builtin_amdgcn_ds_read_tr16_b64_v4i16((LAS s16x4*)p); }
DI bf16x8 lds_tr2(lds_ptr p, int step) {
  const s16x4 lo = lds_tr(p), hi = lds_tr(p + step);
  return __builtin_shufflevector(lo, hi, 0, 1, 2, 3, 4, 5, 6, 7);
}
DI float xhalf_max(float v) {
  auto r = __builtin_amdgcn_permlane32_swap(__float_as_uint(v), __float_as_uint(v), false, false);
  return fmaxf(__uint_as_float(r[0]), __uint_as_float(r[1]));
}
DI float xhalf_sum(float v) {
  auto r = __builtin_amdgcn_permlane32_swap(__float_as_uint(v), __float_as_uint(v), false, false);
  return __uint_as_float(r[0]) + __uint_as_float(r[1]);
}

__global__ __launch_bounds__(512, 2)
void attn_fused(const char* __restrict__ xA, const char* __restrict__ kA, const char* __restrict__ vA,
                const char* __restrict__ wqA, const char* __restrict__ woA,
                const char* __restrict__ xB, const char* __restrict__ kB, const char* __restrict__ vB,
                const char* __restrict__ wqB, const char* __restrict__ woB, char* __restrict__ part) {
  __shared__ __attribute__((aligned(1024))) char smem[LDS_BYTES];
  const lds_ptr lds = (lds_ptr)smem;
  const unsigned lds0 = (unsigned)(uintptr_t)lds;

  const int xcd = blockIdx.x & 7, idx = blockIdx.x >> 3;
  const int br = xcd >> 2, head = (xcd & 3) * 4 + (idx & 3), bat = idx >> 2;
  const int tid = threadIdx.x, lane = tid & 63;
  const int wid = __builtin_amdgcn_readfirstlane(tid >> 6);
  const int r = lane & 31, h = lane >> 5, g = (lane >> 4) & 1, q4 = (lane >> 2) & 3, p = lane & 3;
  const int wl = wid & 3, wh = wid >> 2;

  const char* xg  = (br ? xB : xA) + (size_t)bat * SQ * DM * 2;
  const char* kg  = (br ? kB : kA) + (size_t)(bat * NH + head) * SK * DH * 2;
  const char* vg  = (br ? vB : vA) + (size_t)(bat * NH + head) * SK * DH * 2;
  const char* wqg = (br ? wqB : wqA) + (size_t)head * DH * 2;
  const char* wog = (br ? woB : woA) + (size_t)head * DH * DM * 2;
  const __amdgpu_buffer_rsrc_t rsX  = __builtin_amdgcn_make_buffer_rsrc((void*)xg, 0, SQ * DM * 2, 0x00020000);
  const __amdgpu_buffer_rsrc_t rsK  = __builtin_amdgcn_make_buffer_rsrc((void*)kg, 0, SK * DH * 2, 0x00020000);
  const __amdgpu_buffer_rsrc_t rsV  = __builtin_amdgcn_make_buffer_rsrc((void*)vg, 0, SK * DH * 2, 0x00020000);
  const __amdgpu_buffer_rsrc_t rsWq = __builtin_amdgcn_make_buffer_rsrc((void*)wqg, 0, (DM - 1) * NH * DH * 2 + DH * 2, 0x00020000);
  const __amdgpu_buffer_rsrc_t rsWo = __builtin_amdgcn_make_buffer_rsrc((void*)wog, 0, DH * DM * 2, 0x00020000);

  const unsigned swzT = (unsigned)((r ^ ((2 * (wid & 1) + h) << 2)) << 4);
  const unsigned voWq = (unsigned)h * (NH * DH * 2) + swzT;
  const unsigned voV  = (unsigned)h * 512u + swzT;
  const unsigned voK0 = (unsigned)h * 512u + (unsigned)((r ^ h) << 4);
  const unsigned voX  = (unsigned)(lane >> 3) * (DM * 2) + (unsigned)(((lane & 7) ^ (4 * (wid & 1) + (lane >> 4))) << 4);
  const unsigned voWo = (unsigned)(lane >> 3) * (DM * 2) + (unsigned)(((lane & 7) ^ (g << 2)) << 4);

#define XBUF(j) (SLOT + ((j) & 1) * 49152)
#define WBUF(s) (SLOT + (((s) >> 1) & 1) * 49152 + 16384 + ((s) & 1) * 16384)
#define DMA_X(j) do { _Pragma("unroll") for (int i_ = 0; i_ < 4; ++i_) { const int pc = wl + 4 * i_; \
    dma(rsX, voX, (unsigned)(pc * 8 * DM * 2 + (j) * 128), lds0 + XBUF(j) + pc * 1024); } } while (0)
#define DMA_W(s) do { _Pragma("unroll") for (int i_ = 0; i_ < 4; ++i_) { const int pc = wl + 4 * i_; \
    dma(rsWq, voWq, (unsigned)(((s) * 32 + 2 * pc) * (NH * DH * 2)), lds0 + WBUF(s) + pc * 1024); } } while (0)
#define DMA_K(t, slot) do { _Pragma("unroll") for (int i_ = 0; i_ < 4; ++i_) { const int pc = wid + 8 * i_; \
    dma(rsK, voKw, (unsigned)((t) * SLOT + pc * 1024), lds0 + (slot) * SLOT + pc * 1024); } } while (0)
#define DMA_V(t, slot) do { _Pragma("unroll") for (int i_ = 0; i_ < 4; ++i_) { const int pc = wid + 8 * i_; \
    dma(rsV, voV, (unsigned)((t) * SLOT + pc * 1024), lds0 + (slot) * SLOT + pc * 1024); } } while (0)
#define DMA_WO(j, slot) do { _Pragma("unroll") for (int i_ = 0; i_ < 4; ++i_) { const int pc = wid + 8 * i_; \
    dma(rsWo, voWo, (unsigned)(pc * 8 * DM * 2 + (j) * 128), lds0 + (slot) * SLOT + pc * 1024); } } while (0)

  if (wh == 0) {
    DMA_X(0); DMA_W(0); DMA_W(1); DMA_X(1); DMA_W(2); DMA_W(3);
  } else {
#pragma unroll
    for (int t = 0; t < 2; ++t)
#pragma unroll
      for (int i = 0; i < 8; ++i) { const int pc = wl + 4 * i;
        dma(rsK, voK0 ^ (unsigned)(((2 * pc) & 15) << 4), (unsigned)(t * SLOT + pc * 1024), lds0 + (t == 0 ? 4 : 0) * SLOT + pc * 1024); }
  }
  const unsigned voKw = voK0 ^ (unsigned)(((2 * wid) & 15) << 4);

  const int trA0 = (8 * h + q4) * 512 + (((8 * wl + 2 * g + (p & 1)) ^ (q4 << 2)) << 4) + (p >> 1) * 8;
  const int trA1 = trA0 ^ 64;
  const int fragB = lane * 16;

  f32x16 acc[2][2];
#pragma unroll
  for (int a = 0; a < 2; ++a)
#pragma unroll
    for (int b = 0; b < 2; ++b) acc[a][b] = f32x16{};
  const int xrd = (64 * wh + r) * 128 + ((h ^ ((r >> 1) & 7)) << 4);
#define P1_STAGE(s, NW) do { \
    if (wh == 0) { asm volatile("s_waitcnt vmcnt(" #NW ")" ::: "memory"); } \
    LDS_BAR(); \
    if (wh == 0) { if ((s) >= 1 && (s) <= 10) { if (((s) & 1) == 0) DMA_X((s) / 2 + 1); DMA_W((s) + 3); } } \
    _Pragma("unroll") for (int ks = 0; ks < 2; ++ks) { \
      const int kk = 2 * ((s) & 1) + ks; \
      const bf16x8 a0 = lds_tr2(lds + WBUF(s) + trA0 + ks * 8192, 2048); \
      const bf16x8 a1 = lds_tr2(lds + WBUF(s) + trA1 + ks * 8192, 2048); \
      const bf16x8 b0 = lds_b128(lds + XBUF((s) >> 1) + (xrd ^ (kk << 5))); \
      const bf16x8 b1 = lds_b128(lds + XBUF((s) >> 1) + (xrd ^ (kk << 5)) + 4096); \
      acc[0][0] = MFMA(a0, b0, acc[0][0]); acc[0][1] = MFMA(a0, b1, acc[0][1]); \
      acc[1][0] = MFMA(a1, b0, acc[1][0]); acc[1][1] = MFMA(a1, b1, acc[1][1]); \
    } } while (0)
  P1_STAGE(0, 16); P1_STAGE(1, 12); P1_STAGE(2, 8);  P1_STAGE(3, 12); P1_STAGE(4, 8);  P1_STAGE(5, 12); P1_STAGE(6, 8);
  P1_STAGE(7, 12); P1_STAGE(8, 8);  P1_STAGE(9, 12); P1_STAGE(10, 8); P1_STAGE(11, 12); P1_STAGE(12, 4); P1_STAGE(13, 0);

  LDS_BAR();
  DMA_K(2, 1);
#pragma unroll
  for (int dt = 0; dt < 2; ++dt)
#pragma unroll
    for (int qt = 0; qt < 2; ++qt)
#pragma unroll
      for (int s = 0; s < 2; ++s)
        lds_w128(lds + 2 * SLOT + ((4 * wl + 2 * dt + s) * 4 + 2 * wh + qt) * 1024 + fragB, pack8(acc[dt][qt], s));
  LDS_BAR();
  bf16x8 qf[16];
#pragma unroll
  for (int ks = 0; ks < 16; ++ks) qf[ks] = lds_b128(lds + 2 * SLOT + (ks * 4 + wl) * 1024 + fragB);

  const int kr = (r & 19) | ((r & 4) << 1) | ((r & 8) >> 1);
  const int krd = (32 * wh + kr) * 512 + ((h ^ (kr & 15)) << 4);
  f32x16 S[8];
#define P2_STAGE(t, ISSUE) do { \
    WAIT_BAR(12); \
    ISSUE; \
    f32x16 c = f32x16{}; \
    _Pragma("unroll") for (int ks = 0; ks < 16; ++ks) { \
      const bf16x8 a = lds_b128(lds + (((t) + 4) % 5) * SLOT + (krd ^ ((ks & 7) << 5)) + (ks >> 3) * 256); \
      c = MFMA(a, qf[ks], c); } \
    S[t] = c; } while (0)
  P2_STAGE(0, { DMA_K(3, 2); DMA_K(4, 3); });
  P2_STAGE(1, DMA_K(5, 4));
  P2_STAGE(2, DMA_K(6, 0));
  P2_STAGE(3, DMA_K(7, 1));
  P2_STAGE(4, DMA_V(0, 2));
  P2_STAGE(5, DMA_V(1, 3));
  P2_STAGE(6, DMA_V(2, 4));
  P2_STAGE(7, {});

  float mx = S[0][0];
#pragma unroll
  for (int t = 0; t < 8; ++t)
#pragma unroll
    for (int i = 0; i < 16; ++i) mx = fmaxf(mx, S[t][i]);
  mx = xhalf_max(mx);
  const float mc = mx * SCL;
  float ls = 0.f;
#pragma unroll
  for (int t = 0; t < 8; ++t)
#pragma unroll
    for (int i = 0; i < 16; ++i) { const float e = __builtin_amdgcn_exp2f(fmaf(S[t][i], SCL, -mc)); S[t][i] = e; ls += e; }
  ls = xhalf_sum(ls);
  if (h == 0) *(LAS f32x2*)(lds + ((2 * (1 - wh)) * 4 + wl) * 1024 + r * 8) = (f32x2){mc, ls};
  LDS_BAR();
  float fs;
  { const f32x2 o = *(const LAS f32x2*)(lds + ((2 * wh) * 4 + wl) * 1024 + r * 8);
    const float m = fmaxf(mc, o[0]);
    const float e0 = __builtin_amdgcn_exp2f(mc - m), e1 = __builtin_amdgcn_exp2f(o[0] - m);
    fs = e0 / (ls * e0 + o[1] * e1); }
  u32x4 pf[8][2];
#pragma unroll
  for (int t = 0; t < 8; ++t) {
#pragma unroll
    for (int i = 0; i < 16; ++i) S[t][i] *= fs;
    pf[t][0] = pack8(S[t], 0); pf[t][1] = pack8(S[t], 1);
  }
#pragma unroll
  for (int t = 0; t < 4; ++t)
#pragma unroll
    for (int s = 0; s < 2; ++s) lds_w128(lds + ((4 * t + 2 * wh + s) * 4 + wl) * 1024 + fragB, pf[t][s]);

#pragma unroll
  for (int a = 0; a < 2; ++a)
#pragma unroll
    for (int b = 0; b < 2; ++b) acc[a][b] = f32x16{};
#define P3_BODY(t) do { \
    _Pragma("unroll") for (int ks = 0; ks < 4; ++ks) { \
      const bf16x8 a0 = lds_tr2(lds + (2 + (t) % 3) * SLOT + trA0 + ks * 8192, 2048); \
      const bf16x8 a1 = lds_tr2(lds + (2 + (t) % 3) * SLOT + trA1 + ks * 8192, 2048); \
      const bf16x8 b0 = lds_b128(lds + ((((t) & 3) * 4 + ks) * 4 + 2 * wh) * 1024 + fragB); \
      const bf16x8 b1 = lds_b128(lds + ((((t) & 3) * 4 + ks) * 4 + 2 * wh + 1) * 1024 + fragB); \
      acc[0][0] = MFMA(a0, b0, acc[0][0]); acc[0][1] = MFMA(a0, b1, acc[0][1]); \
      acc[1][0] = MFMA(a1, b0, acc[1][0]); acc[1][1] = MFMA(a1, b1, acc[1][1]); \
    } } while (0)
  WAIT_BAR(8);               P3_BODY(0);
  WAIT_BAR(4); DMA_V(3, 2);  P3_BODY(1);
  WAIT_BAR(4); DMA_V(4, 3);  P3_BODY(2);
  WAIT_BAR(4); DMA_V(5, 4);  P3_BODY(3);
  LDS_BAR();   DMA_V(6, 2);
#pragma unroll
  for (int t = 0; t < 4; ++t)
#pragma unroll
    for (int s = 0; s < 2; ++s) lds_w128(lds + ((4 * t + 2 * wh + s) * 4 + wl) * 1024 + fragB, pf[4 + t][s]);
  WAIT_BAR(8);               P3_BODY(4);
  WAIT_BAR(4); DMA_V(7, 3);  P3_BODY(5);
  WAIT_BAR(4); DMA_WO(0, 4); P3_BODY(6);
  WAIT_BAR(4); DMA_WO(1, 2); P3_BODY(7);

  LDS_BAR();
  DMA_WO(2, 3);
#pragma unroll
  for (int dt = 0; dt < 2; ++dt)
#pragma unroll
    for (int qt = 0; qt < 2; ++qt)
#pragma unroll
      for (int s = 0; s < 2; ++s)
        lds_w128(lds + ((4 * wl + 2 * dt + s) * 4 + 2 * wh + qt) * 1024 + fragB, pack8(acc[dt][qt], s));
  LDS_BAR();
#pragma unroll
  for (int ks = 0; ks < 16; ++ks) qf[ks] = lds_b128(lds + (ks * 4 + wl) * 1024 + fragB);

  const int trW = (8 * h + q4) * 128 + (((4 * wh + 2 * g + (p & 1)) ^ ((q4 >> 1) << 2)) << 4) + (p >> 1) * 8;
  char* pw = part + (size_t)((br * NB + bat) * NH + head) * PART_BYTES + wid * 2048 + lane * 16;
#define P4_BODY(j, slot) do { \
    f32x16 c = f32x16{}; \
    _Pragma("unroll") for (int ks = 0; ks < 16; ++ks) { \
      const bf16x8 a = lds_tr2(lds + (slot) * SLOT + trW + ks * 2048, 512); \
      c = MFMA(a, qf[ks], c); } \
    *(u32x4*)(pw + (j) * 16384) = pack8(c, 0); \
    *(u32x4*)(pw + (j) * 16384 + 1024) = pack8(c, 1); } while (0)
  WAIT_BAR(8);  DMA_WO(3, 0); DMA_WO(4, 1); P4_BODY(0, 4);
  WAIT_BAR(14); DMA_WO(5, 4);               P4_BODY(1, 2);
  WAIT_BAR(16); DMA_WO(6, 2);               P4_BODY(2, 3);
  WAIT_BAR(18);                             P4_BODY(3, 0);
  WAIT_BAR(16);                             P4_BODY(4, 1);
  WAIT_BAR(12);                             P4_BODY(5, 4);
  WAIT_BAR(8);                              P4_BODY(6, 2);
}

__global__ __launch_bounds__(256)
void reduce_heads(const char* __restrict__ part, char* __restrict__ out) {
  const int gid = blockIdx.x * 256 + threadIdx.x;
  const int grp = gid / (PART_BYTES / 16), c = gid % (PART_BYTES / 16);
  const char* src = part + (size_t)grp * NH * PART_BYTES + c * 16;
  u32x4 v[NH];
#pragma unroll
  for (int hh = 0; hh < NH; ++hh) v[hh] = *(const u32x4*)(src + (size_t)hh * PART_BYTES);
  float s[8];
#pragma unroll
  for (int i = 0; i < 8; ++i) s[i] = 0.f;
#pragma unroll
  for (int hh = 0; hh < NH; ++hh)
#pragma unroll
    for (int i = 0; i < 4; ++i) { s[2 * i] += __uint_as_float(v[hh][i] << 16); s[2 * i + 1] += __uint_as_float(v[hh][i] & 0xffff0000u); }
  const int lane = c & 63, sp = (c >> 6) & 1, wid = (c >> 7) & 7, j = c >> 10;
  const int q = 32 * (wid & 3) + (lane & 31), n = 64 * j + 32 * (wid >> 2) + 16 * sp + 8 * (lane >> 5);
  const int br = grp >> 3, bat = grp & 7;
  *(u32x4*)(out + ((size_t)(bat * SQ + q) * (2 * DM) + br * DM + n) * 2) =
      (u32x4){pk(s[0], s[1]), pk(s[2], s[3]), pk(s[4], s[5]), pk(s[6], s[7])};
}

extern "C" void kernel_launch(void* const* d_in, const int* in_sizes, int n_in,
                              void* d_out, int out_size, void* d_ws, size_t ws_size,
                              hipStream_t stream) {
  char* part = (char*)d_ws;
  attn_fused<<<256, 512, 0, stream>>>((const char*)d_in[0], (const char*)d_in[1], (const char*)d_in[2], (const char*)d_in[3], (const char*)d_in[4],
                                      (const char*)d_in[5], (const char*)d_in[6], (const char*)d_in[7], (const char*)d_in[8], (const char*)d_in[9], part);
  reduce_heads<<<2 * NB * (PART_BYTES / 16) / 256, 256, 0, stream>>>(part, (char*)d_out);
}
```

```cpp
#include <hip/hip_runtime.h>
#include <hip/hip_bf16.h>
#include <stdint.h>
#define KVPOL "nt"
#define WPOL ""

typedef short    bf16x8 __attribute__((ext_vector_type(8)));
typedef short    s16x4  __attribute__((ext_vector_type(4)));
typedef float    f32x16 __attribute__((ext_vector_type(16)));
typedef float    f32x2  __attribute__((ext_vector_type(2)));
typedef __bf16   bf16x2 __attribute__((ext_vector_type(2)));
typedef unsigned u32x4  __attribute__((ext_vector_type(4)));
#define LAS __attribute__((address_space(3)))
typedef LAS char* lds_ptr;

constexpr int NB = 8, SQ = 128, DM = 448, NH = 16, SK = 512, DH = 256;
constexpr int SLOT = 32768;
constexpr int LDS_BYTES = 5 * SLOT;
constexpr int PART_BYTES = SQ * DM * 2;
constexpr float SCL = 0.0625f * 1.4426950408889634f;

#define STAMP(i) do {} while (0)
#define STAMPC(i) do {} while (0)
#define DI __device__ __forceinline__
#define MFMA(a, b, c) __builtin_amdgcn_mfma_f32_32x32x16_bf16((a), (b), (c), 0, 0, 0)
#define WAIT_BAR(N) asm volatile("s_waitcnt vmcnt(" #N ") lgkmcnt(0)\n\ts_barrier" ::: "memory")
#define LDS_BAR()   asm volatile("s_waitcnt lgkmcnt(0)\n\ts_barrier" ::: "memory")

DI void dma(__amdgpu_buffer_rsrc_t rs, unsigned voff, unsigned soff, unsigned ldsb) {
  asm volatile("s_mov_b32 m0, %0\n\ts_nop 0\n\tbuffer_load_dwordx4 %1, %2, %3 offen " WPOL " lds"
               :: "s"(ldsb), "v"(voff), "s"(rs), "s"(soff) : "memory");
}
DI void dma_nt(__amdgpu_buffer_rsrc_t rs, unsigned voff, unsigned soff, unsigned ldsb) {
  asm volatile("s_mov_b32 m0, %0\n\ts_nop 0\n\tbuffer_load_dwordx4 %1, %2, %3 offen " KVPOL " lds"
               :: "s"(ldsb), "v"(voff), "s"(rs), "s"(soff) : "memory");
}
DI unsigned pk(float a, float b) {
  const bf16x2 v = __builtin_convertvector((f32x2){a, b}, bf16x2);
  return __builtin_bit_cast(unsigned, v);
}
DI u32x4 pack8(const f32x16& x, int s) {
  return (u32x4){pk(x[8 * s], x[8 * s + 1]), pk(x[8 * s + 2], x[8 * s + 3]), pk(x[8 * s + 4], x[8 * s + 5]), pk(x[8 * s + 6], x[8 * s + 7])};
}
DI bf16x8 lds_b128(lds_ptr p) { return *(const LAS bf16x8*)p; }
DI void lds_w128(lds_ptr p, u32x4 v) { *(LAS u32x4*)p = v; }
DI s16x4 lds_tr(lds_ptr p) { return __builtin_amdgcn_ds_read_tr16_b64_v4i16((LAS s16x4*)p); }
DI bf16x8 lds_tr2(lds_ptr p, int step) {
  const s16x4 lo = lds_tr(p), hi = lds_tr(p + step);
  return __builtin_shufflevector(lo, hi, 0, 1, 2, 3, 4, 5, 6, 7);
}
DI float xhalf_max(float v) {
  auto r = __builtin_amdgcn_permlane32_swap(__float_as_uint(v), __float_as_uint(v), false, false);
  return fmaxf(__uint_as_float(r[0]), __uint_as_float(r[1]));
}
DI float xhalf_sum(float v) {
  auto r = __builtin_amdgcn_permlane32_swap(__float_as_uint(v), __float_as_uint(v), false, false);
  return __uint_as_float(r[0]) + __uint_as_float(r[1]);
}

__global__ __launch_bounds__(512, 2)
void attn_fused(const char* __restrict__ xA, const char* __restrict__ kA, const char* __restrict__ vA,
                const char* __restrict__ wqA, const char* __restrict__ woA,
                const char* __restrict__ xB, const char* __restrict__ kB, const char* __restrict__ vB,
                const char* __restrict__ wqB, const char* __restrict__ woB, char* __restrict__ part) {
  __shared__ __attribute__((aligned(1024))) char smem[LDS_BYTES];
  const lds_ptr lds = (lds_ptr)smem;
  const unsigned lds0 = (unsigned)(uintptr_t)lds;

  const int xcd = blockIdx.x & 7, idx = blockIdx.x >> 3;
  const int br = xcd >> 2, head = (xcd & 3) * 4 + (idx & 3), bat = idx >> 2;
  const int tid = threadIdx.x, lane = tid & 63;
  const int wid = __builtin_amdgcn_readfirstlane(tid >> 6);
  const int r = lane & 31, h = lane >> 5, g = (lane >> 4) & 1, q4 = (lane >> 2) & 3, p = lane & 3;
  const int wl = wid & 3, wh = wid >> 2;

  const char* xg  = (br ? xB : xA) + (size_t)bat * SQ * DM * 2;
  const char* kg  = (br ? kB : kA) + (size_t)(bat * NH + head) * SK * DH * 2;
  const char* vg  = (br ? vB : vA) + (size_t)(bat * NH + head) * SK * DH * 2;
  const char* wqg = (br ? wqB : wqA) + (size_t)head * DH * 2;
  const char* wog = (br ? woB : woA) + (size_t)head * DH * DM * 2;
  const __amdgpu_buffer_rsrc_t rsX  = __builtin_amdgcn_make_buffer_rsrc((void*)xg, 0, SQ * DM * 2, 0x00020000);
  const __amdgpu_buffer_rsrc_t rsK  = __builtin_amdgcn_make_buffer_rsrc((void*)kg, 0, SK * DH * 2, 0x00020000);
  const __amdgpu_buffer_rsrc_t rsV  = __builtin_amdgcn_make_buffer_rsrc((void*)vg, 0, SK * DH * 2, 0x00020000);
  const __amdgpu_buffer_rsrc_t rsWq = __builtin_amdgcn_make_buffer_rsrc((void*)wqg, 0, (DM - 1) * NH * DH * 2 + DH * 2, 0x00020000);
  const __amdgpu_buffer_rsrc_t rsWo = __builtin_amdgcn_make_buffer_rsrc((void*)wog, 0, DH * DM * 2, 0x00020000);

  const unsigned swzT = (unsigned)((r ^ ((2 * (wid & 1) + h) << 2)) << 4);
  const unsigned voWq = (unsigned)h * (NH * DH * 2) + swzT;
  const unsigned voV  = (unsigned)h * 512u + swzT;
  const unsigned voK0 = (unsigned)h * 512u + (unsigned)((r ^ h) << 4);
  const unsigned voX  = (unsigned)(lane >> 3) * (DM * 2) + (unsigned)(((lane & 7) ^ (4 * (wid & 1) + (lane >> 4))) << 4);
  const unsigned voWo = (unsigned)(lane >> 3) * (DM * 2) + (unsigned)(((lane & 7) ^ (g << 2)) << 4);

#define XBUF(j) (SLOT + ((j) & 1) * 49152)
#define WBUF(s) (SLOT + (((s) >> 1) & 1) * 49152 + 16384 + ((s) & 1) * 16384)
#define DMA_X(j) do { _Pragma("unroll") for (int i_ = 0; i_ < 4; ++i_) { const int pc = wl + 4 * i_; \
    dma(rsX, voX, (unsigned)(pc * 8 * DM * 2 + (j) * 128), lds0 + XBUF(j) + pc * 1024); } } while (0)
#define DMA_W(s) do { _Pragma("unroll") for (int i_ = 0; i_ < 4; ++i_) { const int pc = wl + 4 * i_; \
    dma(rsWq, voWq, (unsigned)(((s) * 32 + 2 * pc) * (NH * DH * 2)), lds0 + WBUF(s) + pc * 1024); } } while (0)
#define DMA_K(t, slot) do { _Pragma("unroll") for (int i_ = 0; i_ < 4; ++i_) { const int pc = wid + 8 * i_; \
    dma_nt(rsK, voKw, (unsigned)((t) * SLOT + pc * 1024), lds0 + (slot) * SLOT + pc * 1024); } } while (0)
#define DMA_V(t, slot) do { _Pragma("unroll") for (int i_ = 0; i_ < 4; ++i_) { const int pc = wid + 8 * i_; \
    dma_nt(rsV, voV, (unsigned)((t) * SLOT + pc * 1024), lds0 + (slot) * SLOT + pc * 1024); } } while (0)
#define DMA_WO(j, slot) do { _Pragma("unroll") for (int i_ = 0; i_ < 4; ++i_) { const int pc = wid + 8 * i_; \
    dma(rsWo, voWo, (unsigned)(pc * 8 * DM * 2 + (j) * 128), lds0 + (slot) * SLOT + pc * 1024); } } while (0)

  unsigned stv = 0; (void)stv; STAMP(0); STAMPC(62);
  if (wh == 0) {
    DMA_X(0); DMA_W(0); DMA_W(1); DMA_X(1); DMA_W(2); DMA_W(3);
  } else {
#pragma unroll
    for (int t = 0; t < 2; ++t)
#pragma unroll
      for (int i = 0; i < 8; ++i) { const int pc = wl + 4 * i;
        dma_nt(rsK, voK0 ^ (unsigned)(((2 * pc) & 15) << 4), (unsigned)(t * SLOT + pc * 1024), lds0 + (t == 0 ? 4 : 0) * SLOT + pc * 1024); }
  }
  const unsigned voKw = voK0 ^ (unsigned)(((2 * wid) & 15) << 4);

  const int trA0 = (8 * h + q4) * 512 + (((8 * wl + 2 * g + (p & 1)) ^ (q4 << 2)) << 4) + (p >> 1) * 8;
  const int trA1 = trA0 ^ 64;
  const int fragB = lane * 16;

  f32x16 acc[2][2];
#pragma unroll
  for (int a = 0; a < 2; ++a)
#pragma unroll
    for (int b = 0; b < 2; ++b) acc[a][b] = f32x16{};
  const int xrd = (64 * wh + r) * 128 + ((h ^ ((r >> 1) & 7)) << 4);
#define P1_STAGE(s, NW) do { \
    if (wh == 0) { asm volatile("s_waitcnt vmcnt(" #NW ")" ::: "memory"); } \
    LDS_BAR(); STAMP(1 + (s)); \
    if (wh == 0) { if ((s) >= 1 && (s) <= 10) { if (((s) & 1) == 0) DMA_X((s) / 2 + 1); DMA_W((s) + 3); } } \
    _Pragma("unroll") for (int ks = 0; ks < 2; ++ks) { \
      const int kk = 2 * ((s) & 1) + ks; \
      const bf16x8 a0 = lds_tr2(lds + WBUF(s) + trA0 + ks * 8192, 2048); \
      const bf16x8 a1 = lds_tr2(lds + WBUF(s) + trA1 + ks * 8192, 2048); \
      const bf16x8 b0 = lds_b128(lds + XBUF((s) >> 1) + (xrd ^ (kk << 5))); \
      const bf16x8 b1 = lds_b128(lds + XBUF((s) >> 1) + (xrd ^ (kk << 5)) + 4096); \
      acc[0][0] = MFMA(a0, b0, acc[0][0]); acc[0][1] = MFMA(a0, b1, acc[0][1]); \
      acc[1][0] = MFMA(a1, b0, acc[1][0]); acc[1][1] = MFMA(a1, b1, acc[1][1]); \
    } } while (0)
  P1_STAGE(0, 16); P1_STAGE(1, 12); P1_STAGE(2, 8);  P1_STAGE(3, 12); P1_STAGE(4, 8);  P1_STAGE(5, 12); P1_STAGE(6, 8);
  P1_STAGE(7, 12); P1_STAGE(8, 8);  P1_STAGE(9, 12); P1_STAGE(10, 8); P1_STAGE(11, 12); P1_STAGE(12, 4); P1_STAGE(13, 0);

  LDS_BAR(); STAMP(15);
  DMA_K(2, 1);
#pragma unroll
  for (int dt = 0; dt < 2; ++dt)
#pragma unroll
    for (int qt = 0; qt < 2; ++qt)
#pragma unroll
      for (int s = 0; s < 2; ++s)
        lds_w128(lds + 2 * SLOT + ((4 * wl + 2 * dt + s) * 4 + 2 * wh + qt) * 1024 + fragB, pack8(acc[dt][qt], s));
  LDS_BAR();
  bf16x8 qf[16];
#pragma unroll
  for (int ks = 0; ks < 16; ++ks) qf[ks] = lds_b128(lds + 2 * SLOT + (ks * 4 + wl) * 1024 + fragB);

  const int kr = (r & 19) | ((r & 4) << 1) | ((r & 8) >> 1);
  const int krd = (32 * wh + kr) * 512 + ((h ^ (kr & 15)) << 4);
  f32x16 S[8];
#define P2_STAGE(t, ISSUE) do { \
    WAIT_BAR(12); STAMP(16 + (t)); \
    ISSUE; \
    f32x16 c = f32x16{}; \
    _Pragma("unroll") for (int ks = 0; ks < 16; ++ks) { \
      const bf16x8 a = lds_b128(lds + (((t) + 4) % 5) * SLOT + (krd ^ ((ks & 7) << 5)) + (ks >> 3) * 256); \
      c = MFMA(a, qf[ks], c); } \
    S[t] = c; } while (0)
  P2_STAGE(0, { DMA_K(3, 2); DMA_K(4, 3); });
  P2_STAGE(1, DMA_K(5, 4));
  P2_STAGE(2, DMA_K(6, 0));
  P2_STAGE(3, DMA_K(7, 1));
  P2_STAGE(4, DMA_V(0, 2));
  P2_STAGE(5, DMA_V(1, 3));
  P2_STAGE(6, DMA_V(2, 4));
  P2_STAGE(7, {});

  STAMP(24);
  float mx = S[0][0];
#pragma unroll
  for (int t = 0; t < 8; ++t)
#pragma unroll
    for (int i = 0; i < 16; ++i) mx = fmaxf(mx, S[t][i]);
  mx = xhalf_max(mx);
  const float mc = mx * SCL;
  float ls = 0.f;
#pragma unroll
  for (int t = 0; t < 8; ++t)
#pragma unroll
    for (int i = 0; i < 16; ++i) { const float e = __builtin_amdgcn_exp2f(fmaf(S[t][i], SCL, -mc)); S[t][i] = e; ls += e; }
  ls = xhalf_sum(ls);
  if (h == 0) *(LAS f32x2*)(lds + ((2 * (1 - wh)) * 4 + wl) * 1024 + r * 8) = (f32x2){mc, ls};
  LDS_BAR(); STAMP(25);
  float fs;
  { const f32x2 o = *(const LAS f32x2*)(lds + ((2 * wh) * 4 + wl) * 1024 + r * 8);
    const float m = fmaxf(mc, o[0]);
    const float e0 = __builtin_amdgcn_exp2f(mc - m), e1 = __builtin_amdgcn_exp2f(o[0] - m);
    fs = e0 / (ls * e0 + o[1] * e1); }
  u32x4 pf[8][2];
#pragma unroll
  for (int t = 0; t < 8; ++t) {
#pragma unroll
    for (int i = 0; i < 16; ++i) S[t][i] *= fs;
    pf[t][0] = pack8(S[t], 0); pf[t][1] = pack8(S[t], 1);
  }
#pragma unroll
  for (int t = 0; t < 4; ++t)
#pragma unroll
    for (int s = 0; s < 2; ++s) lds_w128(lds + ((4 * t + 2 * wh + s) * 4 + wl) * 1024 + fragB, pf[t][s]);

#pragma unroll
  for (int a = 0; a < 2; ++a)
#pragma unroll
    for (int b = 0; b < 2; ++b) acc[a][b] = f32x16{};
#define P3_BODY(t) do { STAMP(26 + (t)); \
    _Pragma("unroll") for (int ks = 0; ks < 4; ++ks) { \
      const bf16x8 a0 = lds_tr2(lds + (2 + (t) % 3) * SLOT + trA0 + ks * 8192, 2048); \
      const bf16x8 a1 = lds_tr2(lds + (2 + (t) % 3) * SLOT + trA1 + ks * 8192, 2048); \
      const bf16x8 b0 = lds_b128(lds + ((((t) & 3) * 4 + ks) * 4 + 2 * wh) * 1024 + fragB); \
      const bf16x8 b1 = lds_b128(lds + ((((t) & 3) * 4 + ks) * 4 + 2 * wh + 1) * 1024 + fragB); \
      acc[0][0] = MFMA(a0, b0, acc[0][0]); acc[0][1] = MFMA(a0, b1, acc[0][1]); \
      acc[1][0] = MFMA(a1, b0, acc[1][0]); acc[1][1] = MFMA(a1, b1, acc[1][1]); \
    } } while (0)
  WAIT_BAR(8);               P3_BODY(0);
  WAIT_BAR(4); DMA_V(3, 2);  P3_BODY(1);
  WAIT_BAR(4); DMA_V(4, 3);  P3_BODY(2);
  WAIT_BAR(4); DMA_V(5, 4);  P3_BODY(3);
  LDS_BAR();   DMA_V(6, 2);
#pragma unroll
  for (int t = 0; t < 4; ++t)
#pragma unroll
    for (int s = 0; s < 2; ++s) lds_w128(lds + ((4 * t + 2 * wh + s) * 4 + wl) * 1024 + fragB, pf[4 + t][s]);
  WAIT_BAR(8);               P3_BODY(4);
  WAIT_BAR(4); DMA_V(7, 3);  P3_BODY(5);
  WAIT_BAR(4); DMA_WO(0, 4); P3_BODY(6);
  WAIT_BAR(4); DMA_WO(1, 2); P3_BODY(7);

  LDS_BAR(); STAMP(34);
  DMA_WO(2, 3);
#pragma unroll
  for (int dt = 0; dt < 2; ++dt)
#pragma unroll
    for (int qt = 0; qt < 2; ++qt)
#pragma unroll
      for (int s = 0; s < 2; ++s)
        lds_w128(lds + ((4 * wl + 2 * dt + s) * 4 + 2 * wh + qt) * 1024 + fragB, pack8(acc[dt][qt], s));
  LDS_BAR();
#pragma unroll
  for (int ks = 0; ks < 16; ++ks) qf[ks] = lds_b128(lds + (ks * 4 + wl) * 1024 + fragB);

  const int trW = (8 * h + q4) * 128 + (((4 * wh + 2 * g + (p & 1)) ^ ((q4 >> 1) << 2)) << 4) + (p >> 1) * 8;
  char* pw = part + (size_t)((br * NB + bat) * NH + head) * PART_BYTES + wid * 2048 + lane * 16;
#define P4_BODY(j, slot) do { STAMP(35 + (j)); \
    f32x16 c = f32x16{}; \
    _Pragma("unroll") for (int ks = 0; ks < 16; ++ks) { \
      const bf16x8 a = lds_tr2(lds + (slot) * SLOT + trW + ks * 2048, 512); \
      c = MFMA(a, qf[ks], c); } \
    *(u32x4*)(pw + (j) * 16384) = pack8(c, 0); \
    *(u32x4*)(pw + (j) * 16384 + 1024) = pack8(c, 1); } while (0)
  WAIT_BAR(8);  DMA_WO(3, 0); DMA_WO(4, 1); P4_BODY(0, 4);
  WAIT_BAR(14); DMA_WO(5, 4);               P4_BODY(1, 2);
  WAIT_BAR(16); DMA_WO(6, 2);               P4_BODY(2, 3);
  WAIT_BAR(18);                             P4_BODY(3, 0);
  WAIT_BAR(16);                             P4_BODY(4, 1);
  WAIT_BAR(12);                             P4_BODY(5, 4);
  WAIT_BAR(8);                              P4_BODY(6, 2);
}

__global__ __launch_bounds__(256)
void reduce_heads(const char* __restrict__ part, char* __restrict__ out) {
  const int gid = blockIdx.x * 256 + threadIdx.x;
  const int grp = gid / (PART_BYTES / 16), c = gid % (PART_BYTES / 16);
  const char* src = part + (size_t)grp * NH * PART_BYTES + c * 16;
  u32x4 v[NH];
#pragma unroll
  for (int hh = 0; hh < NH; ++hh) v[hh] = *(const u32x4*)(src + (size_t)hh * PART_BYTES);
  float s[8];
#pragma unroll
  for (int i = 0; i < 8; ++i) s[i] = 0.f;
#pragma unroll
  for (int hh = 0; hh < NH; ++hh)
#pragma unroll
    for (int i = 0; i < 4; ++i) { s[2 * i] += __uint_as_float(v[hh][i] << 16); s[2 * i + 1] += __uint_as_float(v[hh][i] & 0xffff0000u); }
  const int lane = c & 63, sp = (c >> 6) & 1, wid = (c >> 7) & 7, j = c >> 10;
  const int q = 32 * (wid & 3) + (lane & 31), n = 64 * j + 32 * (wid >> 2) + 16 * sp + 8 * (lane >> 5);
  const int br = grp >> 3, bat = grp & 7;
  *(u32x4*)(out + ((size_t)(bat * SQ + q) * (2 * DM) + br * DM + n) * 2) =
      (u32x4){pk(s[0], s[1]), pk(s[2], s[3]), pk(s[4], s[5]), pk(s[6], s[7])};
}

extern "C" void kernel_launch(void* const* d_in, const int* in_sizes, int n_in,
                              void* d_out, int out_size, void* d_ws, size_t ws_size,
                              hipStream_t stream) {
  char* part = (char*)d_ws;
  attn_fused<<<256, 512, 0, stream>>>((const char*)d_in[0], (const char*)d_in[1], (const char*)d_in[2], (const char*)d_in[3], (const char*)d_in[4],
                                      (const char*)d_in[5], (const char*)d_in[6], (const char*)d_in[7], (const char*)d_in[8], (const char*)d_in[9], part);
  reduce_heads<<<2 * NB * (PART_BYTES / 16) / 256, 256, 0, stream>>>(part, (char*)d_out);
}
```

```cpp
#include <hip/hip_runtime.h>
#include <hip/hip_bf16.h>
#include <stdint.h>

typedef short    bf16x8 __attribute__((ext_vector_type(8)));
typedef short    s16x4  __attribute__((ext_vector_type(4)));
typedef float    f32x16 __attribute__((ext_vector_type(16)));
typedef float    f32x2  __attribute__((ext_vector_type(2)));
typedef __bf16   bf16x2 __attribute__((ext_vector_type(2)));
typedef unsigned u32x4  __attribute__((ext_vector_type(4)));
typedef unsigned long long u64;
#define LAS __attribute__((address_space(3)))
#define GAS __attribute__((address_space(1)))

constexpr int NB = 8, SQ = 128, DM = 448, NH = 16, SK = 512, DH = 256;
constexpr int SLOT = 32768;
constexpr int LDS_BYTES = 5 * SLOT;
constexpr int PART_BYTES = SQ * DM * 2;
constexpr int CTRL_BYTES = 16 * 256;
constexpr float SCL = 0.0625f * 1.4426950408889634f;

#define STAMP(i) do {} while (0)
#define STAMPC(i) do {} while (0)
#define ROT1 0
#define WPOL ""

#define DI __device__ __forceinline__
#define MFMA(a, b, c) __builtin_amdgcn_mfma_f32_32x32x16_bf16((a), (b), (c), 0, 0, 0)
#define SBAR() __builtin_amdgcn_sched_barrier(0)
#define PIN(x) asm volatile("" : "+v"(x))
#define PINS(x) asm volatile("" : "+s"(x))
#define RLX_AGENT __ATOMIC_RELAXED, __HIP_MEMORY_SCOPE_AGENT

template <int I> struct IC { static constexpr int v = I; };
template <int I, int N, class F> DI void sfor_(F& f) { if constexpr (I < N) { f(IC<I>{}); sfor_<I + 1, N>(f); } }
template <int N, class F> DI void sfor(F f) { sfor_<0, N>(f); }

template <int N> DI void wait_bar() { static_assert(N >= 0 && N < 64, "vmcnt"); SBAR(); asm volatile("s_waitcnt vmcnt(%0) lgkmcnt(0)\n\ts_barrier" :: "i"(N) : "memory"); SBAR(); }
template <int N> DI void wait_vm()  { static_assert(N >= 0 && N < 64, "vmcnt"); asm volatile("s_waitcnt vmcnt(%0)" :: "i"(N) : "memory"); }
DI void lds_bar() { SBAR(); asm volatile("s_waitcnt lgkmcnt(0)\n\ts_barrier" ::: "memory"); SBAR(); }

DI void dma(__amdgpu_buffer_rsrc_t rs, unsigned voff, unsigned soff, unsigned ldsb) {
  asm volatile("s_mov_b32 m0, %0\n\ts_nop 0\n\tbuffer_load_dwordx4 %1, %2, %3 offen " WPOL " lds"
               :: "s"(ldsb), "v"(voff), "s"(rs), "s"(soff) : "memory");
}
DI void dma_nt(__amdgpu_buffer_rsrc_t rs, unsigned voff, unsigned soff, unsigned ldsb) {
  asm volatile("s_mov_b32 m0, %0\n\ts_nop 0\n\tbuffer_load_dwordx4 %1, %2, %3 offen nt lds"
               :: "s"(ldsb), "v"(voff), "s"(rs), "s"(soff) : "memory");
}
DI u32x4 ld_nt(__amdgpu_buffer_rsrc_t rs, unsigned voff, unsigned soff) {
  u32x4 v; asm volatile("buffer_load_dwordx4 %0, %1, %2, %3 offen nt" : "=v"(v) : "v"(voff), "s"(rs), "s"(soff) : "memory"); return v;
}
DI unsigned pk(float a, float b) {
  const bf16x2 v = __builtin_convertvector((f32x2){a, b}, bf16x2);
  return __builtin_bit_cast(unsigned, v);
}
DI u32x4 pack8(const f32x16& x, int s) {
  return (u32x4){pk(x[8 * s], x[8 * s + 1]), pk(x[8 * s + 2], x[8 * s + 3]), pk(x[8 * s + 4], x[8 * s + 5]), pk(x[8 * s + 6], x[8 * s + 7])};
}
DI bf16x8 rd128(unsigned a) { return *(const LAS bf16x8*)(uintptr_t)a; }
DI void wr128(unsigned a, u32x4 v) { *(LAS u32x4*)(uintptr_t)a = v; }
DI float rd32(unsigned a) { return *(const LAS float*)(uintptr_t)a; }
DI void wr32(unsigned a, float v) { *(LAS float*)(uintptr_t)a = v; }
DI s16x4 rdtr(unsigned a) { return __builtin_amdgcn_ds_read_tr16_b64_v4i16((LAS s16x4*)(uintptr_t)a); }
DI bf16x8 rdtr2(unsigned a, int step) {
  const s16x4 lo = rdtr(a), hi = rdtr(a + step);
  return __builtin_shufflevector(lo, hi, 0, 1, 2, 3, 4, 5, 6, 7);
}
DI float ex2(float v) { return __builtin_amdgcn_exp2f(v); }
DI float max3(float a, float b, float c) { return fmaxf(fmaxf(a, b), c); }
DI f32x2 xhalf(float v) {
  auto r = __builtin_amdgcn_permlane32_swap(__float_as_uint(v), __float_as_uint(v), false, false);
  return (f32x2){__uint_as_float(r[0]), __uint_as_float(r[1])};
}

constexpr int XBUF(int j) { return SLOT + (j & 1) * 49152; }
constexpr int WBUF(int s) { return SLOT + ((s >> 1) & 1) * 49152 + 16384 + (s & 1) * 16384; }
constexpr int KSLOT(int t) { return ((t + 4) % 5) * SLOT; }
constexpr int VSLOT(int t) { return (1 + (t + 1) % 4) * SLOT; }
constexpr int OSLOT(int j) { return ((j + 2) % 5) * SLOT; }
constexpr int KORD(int i) { return (i >> 1) + (i & 1) * 8; }

enum { OP_X, OP_W, OP_K, OP_V, OP_O, OP_ST };
enum { IV_K = 15, IV_SM = 23, IV_V = 24, IV_O = 33 };
struct Op { int iv, pos, kind, idx, pc; };
struct Tab { Op op[192]; int n; };
#define QUOTA {0, 4, 4, 4, 4, 4, 4, 4, 4, 4, 4, 4, 4, 4, 4, 4, 4, 0}
constexpr int NLAND = 3;
constexpr int avail(int s) {
  return s < 6 ? IV_K + 1 + s : s == 6 ? IV_SM : IV_V + s - 6; }
constexpr Tab make_tab() {
  Tab t{}; int n = 0;
  auto add = [&](int iv, int pos, int kind, int idx, int pc) { t.op[n] = Op{iv, pos, kind, idx, pc}; ++n; };
  for (int i = 0; i < 2; ++i) add(-1, 0, OP_X, 0, i);
  for (int i = 0; i < 4; ++i) add(-1, i >> 1, OP_W, i >> 1, i & 1);
  for (int i = 0; i < 2; ++i) add(-1, 1, OP_X, 1, i);
  for (int i = 0; i < 4; ++i) add(0, 4 + i, OP_W, 2 + (i >> 1), i & 1);
  int kp = 0;
  for (int sn = 1; sn <= 13; ++sn) {
    if (sn <= 10 && (sn & 1) == 0) { add(sn, 2, OP_X, sn / 2 + 1, 0); add(sn, 3, OP_X, sn / 2 + 1, 1); add(sn, 6, OP_W, sn + 3, 0); add(sn, 7, OP_W, sn + 3, 1); }
    else if (sn <= 10) { add(sn, 2, OP_W, sn + 3, 0); add(sn, 3, OP_W, sn + 3, 1); add(sn, 6, OP_K, kp >> 2, kp & 3); ++kp; add(sn, 7, OP_K, kp >> 2, kp & 3); ++kp; }
    else { const int c = sn == 11 ? 4 : 3; const int ps[4] = {2, 3, 6, 7}; for (int i = 0; i < c; ++i) { add(sn, ps[i], OP_K, kp >> 2, kp & 3); ++kp; } }
  }
  const int quota[18] = QUOTA;
  int sp = 0;
  for (int iv = IV_K; iv <= 32; ++iv) {
    const int q = quota[iv - IV_K], p0 = iv == IV_V ? 6 : 2, p1 = (iv == IV_SM || iv == 32) ? 3 : 16;
    for (int i = 0; i < q && sp < 56 && avail(sp >> 2) <= iv; ++i, ++sp) {
      const int s = sp >> 2, pos = iv == IV_SM ? 0 : p0 + (i * (p1 - p0)) / q;
      add(iv, pos, s < 3 ? OP_K : s < 11 ? OP_V : OP_O, s < 3 ? s + 5 : s < 11 ? s - 3 : s - 11, sp & 3);
    }
  }
  for (int j = 1; j <= 6; ++j) {
    if (j <= 2) for (int i = 0; i < 8; ++i) add(IV_O + j, i, OP_O, 1 + 2 * j + (i >> 2), i & 3);
    add(IV_O + j, 9, OP_ST, j - 1, 0); add(IV_O + j, 11, OP_ST, j - 1, 1);
  }
  t.n = n; return t;
}
constexpr Tab TAB = make_tab();
constexpr int op_first(int iv, int pos) { int i = 0; while (i < TAB.n && (TAB.op[i].iv < iv || (TAB.op[i].iv == iv && TAB.op[i].pos < pos))) ++i; return i; }
constexpr int inflight(int iv, int pos, int kind, int idx) {
  int last = -1, cnt = 0; for (int i = 0; i < TAB.n; ++i) if (TAB.op[i].kind == kind && TAB.op[i].idx == idx) { last = i; ++cnt; }
  const int lim = op_first(iv, pos);
  return (cnt != ((kind == OP_X || kind == OP_W) ? 2 : 4) || last >= lim) ? -1 : lim - 1 - last; }
constexpr int cmin(int a, int b) { return a < b ? a : b; }

__global__ __launch_bounds__(512, 2)
void attn_fused(const char* __restrict__ xA, const char* __restrict__ kA, const char* __restrict__ vA,
                const char* __restrict__ wqA, const char* __restrict__ woA,
                const char* __restrict__ xB, const char* __restrict__ kB, const char* __restrict__ vB,
                const char* __restrict__ wqB, const char* __restrict__ woB, char* ws, char* out) {
  __shared__ __attribute__((aligned(1024))) char smem[LDS_BYTES];
  const unsigned lds0 = (unsigned)(uintptr_t)(LAS char*)smem;

  const int xcd = blockIdx.x & 7, idx = blockIdx.x >> 3;
  const int br = xcd >> 2, head = (xcd & 3) * 4 + (idx & 3), bat = idx >> 2, grp = br * NB + bat;
  const int tid = threadIdx.x, lane = tid & 63;
  const int wid = __builtin_amdgcn_readfirstlane(tid >> 6);
  const int r = lane & 31, h = lane >> 5, g = (lane >> 4) & 1, q4 = (lane >> 2) & 3, p = lane & 3;
  const int wl = wid & 3, wh = wid >> 2;
  unsigned stv = 0; (void)stv; STAMP(0); STAMPC(62);

  const char* xg  = (br ? xB : xA) + (size_t)bat * SQ * DM * 2;
  const char* kg  = (br ? kB : kA) + (size_t)(bat * NH + head) * SK * DH * 2;
  const char* vg  = (br ? vB : vA) + (size_t)(bat * NH + head) * SK * DH * 2;
  const char* wqg = (br ? wqB : wqA) + (size_t)head * DH * 2;
  const char* wog = (br ? woB : woA) + (size_t)head * DH * DM * 2;
  const __amdgpu_buffer_rsrc_t rsX  = __builtin_amdgcn_make_buffer_rsrc((void*)xg, 0, SQ * DM * 2, 0x00020000);
  const __amdgpu_buffer_rsrc_t rsK  = __builtin_amdgcn_make_buffer_rsrc((void*)kg, 0, SK * DH * 2, 0x00020000);
  const __amdgpu_buffer_rsrc_t rsV  = __builtin_amdgcn_make_buffer_rsrc((void*)vg, 0, SK * DH * 2, 0x00020000);
  const __amdgpu_buffer_rsrc_t rsWq = __builtin_amdgcn_make_buffer_rsrc((void*)wqg, 0, (DM - 1) * NH * DH * 2 + DH * 2, 0x00020000);
  const __amdgpu_buffer_rsrc_t rsWo = __builtin_amdgcn_make_buffer_rsrc((void*)wog, 0, DH * DM * 2, 0x00020000);
  const __amdgpu_buffer_rsrc_t rsP  = __builtin_amdgcn_make_buffer_rsrc((void*)(ws + CTRL_BYTES + (size_t)grp * NH * PART_BYTES), 0, NH * PART_BYTES, 0x00020000);

  const unsigned swzT = (unsigned)((r ^ ((2 * (wid & 1) + h) << 2)) << 4);
  const unsigned voWq = (unsigned)h * (NH * DH * 2) + swzT;
  const unsigned voV  = (unsigned)h * 512u + swzT;
  const unsigned voK  = (unsigned)h * 512u + (unsigned)((r ^ h ^ ((2 * wid) & 15)) << 4);
  const unsigned voX  = (unsigned)(lane >> 3) * (DM * 2) + (unsigned)(((lane & 7) ^ (4 * (wid & 1) + (lane >> 4))) << 4);
  unsigned voWo = 0;

  const int rot1 = ROT1, rot4 = bat % 7;
  auto rotc = [](int j, int rot) { const int c = j + rot; return c >= 7 ? c - 7 : c; };
  const unsigned ldw = lds0 + wid * 1024;
  const unsigned frag = lds0 + lane * 16;
  unsigned so = 0;
  u32x4 kland[NLAND * 4];
  f32x16 C[7];
  float inv = 0.f;
  const unsigned pw = (unsigned)(head * PART_BYTES + wid * 2048 + lane * 16);

  auto issue = [&](auto IV, auto POS) {
    constexpr int f = op_first(IV.v, POS.v), c = op_first(IV.v, POS.v + 1) - f;
    sfor<c>([&](auto I) { constexpr Op o = TAB.op[f + I.v];
      if constexpr (o.kind == OP_X) {
        if constexpr (o.pc == 0) { so = (unsigned)(wid * 8 * DM * 2 + rotc(o.idx, rot1) * 128); PINS(so); }
        dma(rsX, voX, so + o.pc * (64 * DM * 2), ldw + XBUF(o.idx) + o.pc * 8192);
      } else if constexpr (o.kind == OP_W) {
        if constexpr (o.pc == 0) { so = (unsigned)((rotc(o.idx >> 1, rot1) * 64 + (o.idx & 1) * 32 + 2 * wid) * (NH * DH * 2)); PINS(so); }
        dma(rsWq, voWq, so + o.pc * 16 * (NH * DH * 2), ldw + WBUF(o.idx) + o.pc * 8192);
      } else if constexpr (o.kind == OP_K) {
        if constexpr (o.idx >= 2 && o.idx < 2 + NLAND) kland[(o.idx - 2) * 4 + o.pc] = ld_nt(rsK, voK, (unsigned)(wid * 1024 + o.idx * SLOT + o.pc * 8192));
        else dma_nt(rsK, voK, (unsigned)(wid * 1024 + o.idx * SLOT + o.pc * 8192), ldw + KSLOT(o.idx) + o.pc * 8192);
      } else if constexpr (o.kind == OP_V) {
        dma_nt(rsV, voV, (unsigned)(wid * 1024 + o.idx * SLOT + o.pc * 8192), ldw + VSLOT(o.idx) + o.pc * 8192);
      } else if constexpr (o.kind == OP_O) {
        if constexpr (o.pc == 0) { so = (unsigned)(wid * 8 * DM * 2 + rotc(o.idx, rot4) * 128); PINS(so); }
        dma(rsWo, voWo, so + o.pc * (64 * DM * 2), ldw + OSLOT(o.idx) + o.pc * 8192);
      } else {
        f32x16 x = C[o.idx];
        sfor<8>([&](auto e) { x[8 * o.pc + e.v] *= inv; });
        __builtin_amdgcn_raw_buffer_store_b128(pack8(x, o.pc), rsP, pw + o.idx * 16384 + o.pc * 1024, 0, 16  );
      } }); };
  auto park = [&](auto T, auto I) { wr128(frag + wid * 1024 + KSLOT(T.v) + I.v * 8192, kland[(T.v - 2) * 4 + I.v]); };

  issue(IC<-1>{}, IC<0>{});
  lds_bar();
  issue(IC<-1>{}, IC<1>{});

  const unsigned trA = lds0 + (8 * h + q4) * 512 + (((8 * wl + 2 * g + (p & 1)) ^ (q4 << 2)) << 4) + (p >> 1) * 8;

  f32x16 acc[2][2];
  sfor<4>([&](auto i) { acc[i.v >> 1][i.v & 1] = f32x16{}; });
  auto putT = [&](int base, auto I) { constexpr int dt = I.v >> 1, qt = I.v & 1;
    sfor<2>([&](auto s) { wr128(frag + base + ((4 * wl + 2 * dt + s.v) * 4 + 2 * wh + qt) * 1024, pack8(acc[dt][qt], s.v)); }); };
  {
    const unsigned xrd = lds0 + (64 * wh + r) * 128 + ((h ^ ((r >> 1) & 7)) << 4);
    bf16x8 a0[29], a1[29], b0[29], b1[29];
    auto rdA = [&](auto U) { constexpr int u = U.v, s = u >> 1, ks = u & 1, kk = u & 3;
      a0[u] = rdtr2(trA + WBUF(s) + ks * 8192, 2048); b0[u] = rd128((xrd ^ (kk << 5)) + XBUF(s >> 1)); };
    auto rdB = [&](auto U) { constexpr int u = U.v, s = u >> 1, ks = u & 1, kk = u & 3;
      b1[u] = rd128((xrd ^ (kk << 5)) + XBUF(s >> 1) + 4096); a1[u] = rdtr2((trA ^ 64) + WBUF(s) + ks * 8192, 2048); };
    wait_bar<cmin(inflight(0, 0, OP_X, 0), inflight(0, 0, OP_W, 0))>(); STAMP(1);
    rdA(IC<0>{}); rdB(IC<0>{}); SBAR();
    sfor<28>([&](auto U) { constexpr int u = U.v;
      constexpr int sn = (u + 1) >> 1, k0 = ((u + 1) & 1) * 4;
      if constexpr ((u & 1) == 1) {
        if constexpr (sn < 14) wait_bar<cmin(inflight(sn, 0, OP_X, sn >> 1), inflight(sn, 0, OP_W, sn < 14 ? sn : 0))>();
        else wait_bar<inflight(14, 0, OP_K, 2)>();
        STAMP(1 + sn);
      }
      acc[0][0] = MFMA(a0[u], b0[u], acc[0][0]); SBAR();
      if constexpr (u + 1 < 28) rdA(IC<u + 1>{}); else { park(IC<2>{}, IC<0>{}); park(IC<2>{}, IC<1>{}); }
      issue(IC<sn>{}, IC<k0>{}); SBAR();
      acc[0][1] = MFMA(a0[u], b1[u], acc[0][1]); SBAR();
      if constexpr (u + 1 < 28) rdB(IC<u + 1>{}); else { park(IC<2>{}, IC<2>{}); park(IC<2>{}, IC<3>{}); }
      issue(IC<sn>{}, IC<k0 + 1>{}); SBAR();
      acc[1][0] = MFMA(a1[u], b0[u], acc[1][0]); SBAR(); issue(IC<sn>{}, IC<k0 + 2>{});
      if constexpr (u == 27) putT(2 * SLOT, IC<0>{});
      SBAR();
      acc[1][1] = MFMA(a1[u], b1[u], acc[1][1]); SBAR(); issue(IC<sn>{}, IC<k0 + 3>{}); SBAR();
    });
  }
  putT(2 * SLOT, IC<1>{}); putT(2 * SLOT, IC<2>{}); putT(2 * SLOT, IC<3>{});

  bf16x8 qf[16];
  f32x16 S[8];
  float mh[8], ls = 0.f, mx = 0.f, nm = 0.f;
  auto sm = [&](auto T, auto K) { constexpr int t = T.v, k = K.v;
    if constexpr (k == 0) {
      float a = max3(S[t][0], S[t][1], S[t][2]), b = max3(S[t][3], S[t][4], S[t][5]);
      a = max3(a, S[t][6], S[t][7]); b = max3(b, S[t][8], S[t][9]);
      a = max3(a, S[t][10], S[t][11]); b = max3(b, S[t][12], S[t][13]);
      mx = fmaxf(max3(a, S[t][14], S[t][15]), b); PIN(mx);
    } else if constexpr (k == 1) {
      if constexpr (t == 0) { mh[0] = mx * SCL; }
      else { mh[t] = fmaxf(mh[t > 0 ? t - 1 : 0], mx * SCL); ls *= ex2(mh[t > 0 ? t - 1 : 0] - mh[t]); PIN(ls); }
      nm = -mh[t]; PIN(nm);
    } else {
      constexpr int e = 2 * (k - 2);
      float e0 = ex2(fmaf(S[t][e], SCL, nm)), e1 = ex2(fmaf(S[t][e + 1], SCL, nm));
      ls += e0; ls += e1; PIN(ls); PIN(e0); PIN(e1);
      S[t][e] = e0; S[t][e + 1] = e1;
    } };
  {
    const int kr = (r & 19) | ((r & 4) << 1) | ((r & 8) >> 1);
    const unsigned krd = lds0 + (32 * wh + kr) * 512 + ((h ^ (kr & 15)) << 4);
    bf16x8 af[128];
    unsigned kb = 0;
    auto rd = [&](auto T, auto I) { constexpr int t = T.v, ks = KORD(I.v);
      if constexpr (t == 0) qf[ks] = rd128(frag + 2 * SLOT + (ks * 4 + wl) * 1024);
      af[16 * t + I.v] = rd128((kb ^ ((ks & 7) << 5)) + (ks >> 3) * 256); };
    auto mm = [&](auto T, auto I) { constexpr int t = T.v, i = I.v;
      if constexpr (i == 0) S[t] = MFMA(af[16 * t], qf[KORD(0)], f32x16{}); else S[t] = MFMA(af[16 * t + i], qf[KORD(i)], S[t]); };
    sfor<8>([&](auto T) { constexpr int t = T.v;
      wait_bar<cmin(inflight(IV_K + t, 0, OP_K, t), inflight(IV_K + t, 0, OP_K, (t == 1 || t == 2) ? t + 2 : t))>(); STAMP(16 + t);
      kb = krd + KSLOT(t); PIN(kb);
      sfor<16>([&](auto K) { constexpr int k = K.v;
        if constexpr (k < 3) { if constexpr (t > 0) mm(IC<(t > 0 ? t - 1 : 0)>{}, IC<13 + k>{}); }
        else mm(T, IC<k - 3>{});
        SBAR();
        if constexpr (k < 2) { rd(T, IC<2 * k>{}); rd(T, IC<2 * k + 1>{}); }
        else if constexpr (k < 14) rd(T, IC<k + 2>{});
        issue(IC<IV_K + t>{}, K);
        if constexpr ((t == 1 || t == 2) && k >= 2 && k < 6) park(IC<(t == 1 || t == 2) ? t + 2 : 2>{}, IC<(k - 2) & 3>{});
        if constexpr (t > 0 && k >= 5 && k < 15) sm(IC<(t > 0 ? t - 1 : 0)>{}, IC<k - 5>{});
        SBAR();
      });
    });
    mm(IC<7>{}, IC<13>{}); mm(IC<7>{}, IC<14>{}); mm(IC<7>{}, IC<15>{});
    STAMP(24);
  }
  sm(IC<7>{}, IC<0>{});
  {
    const f32x2 m2 = xhalf(fmaxf(mh[6], mx * SCL));
    const float mw = fmaxf(m2[0], m2[1]);
    if (h == 0) wr32(lds0 + ((2 * (1 - wh)) * 4 + wl) * 1024 + r * 4, mw);
    lds_bar(); STAMP(25);
    mh[7] = fmaxf(mw, rd32(lds0 + ((2 * wh) * 4 + wl) * 1024 + r * 4));
    nm = -mh[7];
  }
  float fs = 1.f; u32x4 pv;
  auto putP = [&](auto T, auto Q) { constexpr int t = T.v, q = Q.v, e = 4 * q;
    if constexpr (q == 0 && t < 7) { fs = ex2(mh[t] + nm); if constexpr (t == 6) { } }
    if constexpr (t < 7) { pv[2 * (q & 1)] = pk(S[t][e] * fs, S[t][e + 1] * fs); pv[2 * (q & 1) + 1] = pk(S[t][e + 2] * fs, S[t][e + 3] * fs); }
    else { pv[2 * (q & 1)] = pk(S[t][e], S[t][e + 1]); pv[2 * (q & 1) + 1] = pk(S[t][e + 2], S[t][e + 3]); }
    if constexpr (q & 1) wr128(frag + (t & 1) * 16384 + ((2 * wh + (q >> 1)) * 4 + wl) * 1024, pv); else PIN(pv); };
  sfor<4>([&](auto q) { putP(IC<0>{}, q); });
  issue(IC<IV_SM>{}, IC<0>{});
  voWo = (unsigned)(lane >> 3) * (DM * 2) + (unsigned)(((lane & 7) ^ (g << 2)) << 4);

  sfor<4>([&](auto i) { acc[i.v >> 1][i.v & 1] = f32x16{}; });
  {
    bf16x8 a0[33], a1[33], b0[33], b1[33];
    unsigned vb = 0;
    auto rdA = [&](auto U) { constexpr int u = U.v, t = u >> 2, ks = u & 3;
      a0[u] = rdtr2(vb + ks * 8192, 2048); b0[u] = rd128(frag + (t & 1) * 16384 + (ks * 4 + 2 * wh) * 1024); };
    auto rdB = [&](auto U) { constexpr int u = U.v, t = u >> 2, ks = u & 3;
      b1[u] = rd128(frag + (t & 1) * 16384 + (ks * 4 + 2 * wh + 1) * 1024); a1[u] = rdtr2((vb ^ 64) + ks * 8192, 2048); };
    wait_bar<inflight(IV_V, 0, OP_V, 0)>(); STAMP(26);
    vb = trA + VSLOT(0); PIN(vb);
    rdA(IC<0>{}); rdB(IC<0>{}); SBAR();
    sfor<32>([&](auto U) { constexpr int u = U.v;
      constexpr int tn = (u + 1) >> 2, gi = (u + 1) & 3;
      if constexpr (gi == 0) {
        if constexpr (tn < 8) { wait_bar<inflight(IV_V + tn, 0, OP_V, tn < 8 ? tn : 0)>(); vb = trA + VSLOT(tn); PIN(vb); } else lds_bar();
        STAMP(26 + tn);
      }
      auto side = [&](auto Z) { constexpr int z = 2 * gi + Z.v;
        if constexpr (tn < 7 && z >= 2 && z < 6) putP(IC<(tn < 7 ? tn + 1 : 0)>{}, IC<(z - 2) & 3>{});
        if constexpr (tn == 1 && z == 0) { ls *= ex2(mh[6] + nm); PIN(ls); }
        if constexpr ((tn == 2 || tn == 3) && (z < 2 || z >= 6)) sm(IC<7>{}, IC<2 + 4 * (tn - 2) + (z < 2 ? z : z - 4)>{});
        if constexpr (tn == 7 && z == 0) { const f32x2 l2 = xhalf(ls); ls = l2[0] + l2[1]; if (h == 0) wr32(lds0 + wid * 128 + r * 4, ls); }
        if constexpr (tn == 7 && z == 3) lds_bar();
        if constexpr (tn == 7 && z == 4) { inv = 1.0f / (ls + rd32(lds0 + (wid ^ 4) * 128 + r * 4)); PIN(inv); }
        if constexpr (u == 31 && Z.v == 0) putT(0, IC<0>{}); };
      acc[0][0] = MFMA(a0[u], b0[u], acc[0][0]); SBAR();
      if constexpr (u + 1 < 32) rdA(IC<u + 1>{});
      issue(IC<IV_V + tn>{}, IC<4 * gi>{}); SBAR();
      acc[0][1] = MFMA(a0[u], b1[u], acc[0][1]); SBAR();
      if constexpr (u + 1 < 32) rdB(IC<u + 1>{});
      issue(IC<IV_V + tn>{}, IC<4 * gi + 1>{}); SBAR();
      acc[1][0] = MFMA(a1[u], b0[u], acc[1][0]); SBAR();
      issue(IC<IV_V + tn>{}, IC<4 * gi + 2>{}); side(IC<0>{}); SBAR();
      acc[1][1] = MFMA(a1[u], b1[u], acc[1][1]); SBAR();
      issue(IC<IV_V + tn>{}, IC<4 * gi + 3>{}); side(IC<1>{}); SBAR();
    });
  }
  putT(0, IC<1>{}); putT(0, IC<2>{}); putT(0, IC<3>{});

  {
    const unsigned trW = lds0 + (8 * h + q4) * 128 + (((4 * wh + 2 * g + (p & 1)) ^ ((q4 >> 1) << 2)) << 4) + (p >> 1) * 8;
    bf16x8 af[112];
    unsigned wb = 0;
    auto rd = [&](auto J, auto I) { constexpr int j = J.v, ks = I.v;
      if constexpr (j == 0) qf[ks] = rd128(frag + (ks * 4 + wl) * 1024);
      af[16 * j + ks] = rdtr2(wb + ks * 2048, 512); };
    auto mm = [&](auto J, auto I) { constexpr int j = J.v, i = I.v;
      if constexpr (i == 0) C[j] = MFMA(af[16 * j], qf[0], f32x16{}); else C[j] = MFMA(af[16 * j + i], qf[i], C[j]); };
    sfor<7>([&](auto J) { constexpr int j = J.v;
      wait_bar<inflight(IV_O + j, 0, OP_O, j)>(); STAMP(35 + j);
      wb = trW + OSLOT(j); PIN(wb);
      sfor<16>([&](auto K) { constexpr int k = K.v;
        if constexpr (k < 3) { if constexpr (j > 0) mm(IC<(j > 0 ? j - 1 : 0)>{}, IC<13 + k>{}); }
        else mm(J, IC<k - 3>{});
        SBAR();
        if constexpr (k < 2) { rd(J, IC<2 * k>{}); rd(J, IC<2 * k + 1>{}); }
        else if constexpr (k < 14) rd(J, IC<k + 2>{});
        issue(IC<IV_O + j>{}, K);
        SBAR();
      });
    });
    mm(IC<6>{}, IC<13>{}); mm(IC<6>{}, IC<14>{}); mm(IC<6>{}, IC<15>{});
    sfor<16>([&](auto e) { C[6][e.v] *= inv; });
    sfor<2>([&](auto s) { __builtin_amdgcn_raw_buffer_store_b128(pack8(C[6], s.v), rsP, pw + 6 * 16384 + s.v * 1024, 0, 16); });
  }
  STAMP(42);

  GAS u64* ctrl = (GAS u64*)(ws + grp * 256);
  const u64 epoch = __hip_atomic_load(ctrl, RLX_AGENT);
  const u64 tag = epoch * 0x9E3779B97F4A7C15ull + 0x632BE59BD9B4E019ull;
  wait_bar<0>();
  STAMP(43);
  if (tid == 0) __hip_atomic_store(ctrl + 16 + head, tag, RLX_AGENT);
  if (wid < 7) {
    const unsigned off = (unsigned)((wid * 16 + head) * 1024 + lane * 16);
    u32x4 v[NH];
    sfor<NH>([&](auto i) { v[i.v] = (u32x4){0u, 0u, 0u, 0u}; });
    unsigned done = 0;
    for (unsigned spins = 0; spins < (1u << 18); ++spins) {
      const u64 f = __hip_atomic_load(ctrl + 16 + (lane & 15), RLX_AGENT);
      const unsigned now = ((unsigned)__ballot(f == tag) & 0xffffu) | (1u << head), fresh = now & ~done;
      __builtin_amdgcn_fence(__ATOMIC_ACQUIRE, "wavefront");
      sfor<NH>([&](auto i) { if (fresh & (1u << i.v)) v[i.v] = __builtin_amdgcn_raw_buffer_load_b128(rsP, off, i.v * PART_BYTES, 16); });
      done |= now;
      if (done == 0xffffu) break;
      __builtin_amdgcn_s_sleep(2);
    }
    STAMP(44);
    if (tid == 0 && head == 0) __hip_atomic_store(ctrl, tag, RLX_AGENT);
    float s[8];
    sfor<8>([&](auto i) { s[i.v] = 0.f; });
    sfor<NH>([&](auto hh) { sfor<4>([&](auto i) {
      s[2 * i.v] += __uint_as_float(v[hh.v][i.v] << 16); s[2 * i.v + 1] += __uint_as_float(v[hh.v][i.v] & 0xffff0000u); }); });
    const int q = 32 * ((head >> 1) & 3) + r, n = 64 * rotc(wid, rot4) + 32 * (head >> 3) + 16 * (head & 1) + 8 * h;
    *(u32x4*)(out + ((size_t)(bat * SQ + q) * (2 * DM) + br * DM + n) * 2) =
        (u32x4){pk(s[0], s[1]), pk(s[2], s[3]), pk(s[4], s[5]), pk(s[6], s[7])};
    STAMP(45);
  }
}

extern "C" void kernel_launch(void* const* d_in, const int* in_sizes, int n_in,
                              void* d_out, int out_size, void* d_ws, size_t ws_size,
                              hipStream_t stream) {
  attn_fused<<<256, 512, 0, stream>>>((const char*)d_in[0], (const char*)d_in[1], (const char*)d_in[2], (const char*)d_in[3], (const char*)d_in[4],
                                      (const char*)d_in[5], (const char*)d_in[6], (const char*)d_in[7], (const char*)d_in[8], (const char*)d_in[9],
                                      (char*)d_ws, (char*)d_out);
}
```

```cpp
#include <hip/hip_runtime.h>
#include <hip/hip_bf16.h>
#include <stdint.h>

typedef short    bf16x8 __attribute__((ext_vector_type(8)));
typedef short    s16x4  __attribute__((ext_vector_type(4)));
typedef float    f32x16 __attribute__((ext_vector_type(16)));
typedef float    f32x2  __attribute__((ext_vector_type(2)));
typedef __bf16   bf16x2 __attribute__((ext_vector_type(2)));
typedef unsigned u32x4  __attribute__((ext_vector_type(4)));
typedef unsigned long long u64;
#define LAS __attribute__((address_space(3)))
#define GAS __attribute__((address_space(1)))

constexpr int NB = 8, SQ = 128, DM = 448, NH = 16, SK = 512, DH = 256;
constexpr int SLOT = 32768;
constexpr int LDS_BYTES = 5 * SLOT;
constexpr int PART_BYTES = SQ * DM * 2;
constexpr int GRP_BYTES = NH * PART_BYTES + 4096;
constexpr int CTRL_BYTES = 16 * 256 + 4096;
constexpr float SCL = 0.0625f * 1.4426950408889634f;

#define STAMP(i) do {} while (0)
#define STAMPC(i) do {} while (0)
#define ROT1 0
#define ROT4 (bat % 7)
#define OPOL ""
#define STAUX 16
#define LDAUX 16
#define WSOFF 0
#define PFIV -9
#define WPOL ""

#define DI __device__ __forceinline__
#define MFMA(a, b, c) __builtin_amdgcn_mfma_f32_32x32x16_bf16((a), (b), (c), 0, 0, 0)
#define SBAR() __builtin_amdgcn_sched_barrier(0)
#define PIN(x) asm volatile("" : "+v"(x))
#define PRIO(n) __builtin_amdgcn_s_setprio(n)
#define PINS(x) asm volatile("" : "+s"(x))
#define RLX_AGENT __ATOMIC_RELAXED, __HIP_MEMORY_SCOPE_AGENT

template <int I> struct IC { static constexpr int v = I; };
template <int I, int N, class F> DI void sfor_(F& f) { if constexpr (I < N) { f(IC<I>{}); sfor_<I + 1, N>(f); } }
template <int N, class F> DI void sfor(F f) { sfor_<0, N>(f); }

template <int N> DI void wait_bar() { static_assert(N >= 0 && N < 64, "vmcnt"); SBAR(); asm volatile("s_waitcnt vmcnt(%0) lgkmcnt(0)\n\ts_barrier" :: "i"(N) : "memory"); SBAR(); }
template <int N> DI void wait_vm()  { static_assert(N >= 0 && N < 64, "vmcnt"); asm volatile("s_waitcnt vmcnt(%0)" :: "i"(N) : "memory"); }
DI void lds_bar() { SBAR(); asm volatile("s_waitcnt lgkmcnt(0)\n\ts_barrier" ::: "memory"); SBAR(); }

DI void dma(__amdgpu_buffer_rsrc_t rs, unsigned voff, unsigned soff, unsigned ldsb) {
  asm volatile("s_mov_b32 m0, %0\n\ts_nop 0\n\tbuffer_load_dwordx4 %1, %2, %3 offen " WPOL " lds"
               :: "s"(ldsb), "v"(voff), "s"(rs), "s"(soff) : "memory");
}
DI void dma_o(__amdgpu_buffer_rsrc_t rs, unsigned voff, unsigned soff, unsigned ldsb) {
  asm volatile("s_mov_b32 m0, %0\n\ts_nop 0\n\tbuffer_load_dwordx4 %1, %2, %3 offen " OPOL " lds"
               :: "s"(ldsb), "v"(voff), "s"(rs), "s"(soff) : "memory");
}
DI void dma_nt(__amdgpu_buffer_rsrc_t rs, unsigned voff, unsigned soff, unsigned ldsb) {
  asm volatile("s_mov_b32 m0, %0\n\ts_nop 0\n\tbuffer_load_dwordx4 %1, %2, %3 offen nt lds"
               :: "s"(ldsb), "v"(voff), "s"(rs), "s"(soff) : "memory");
}
DI u32x4 ld_nt(__amdgpu_buffer_rsrc_t rs, unsigned voff, unsigned soff) {
  u32x4 v; asm volatile("buffer_load_dwordx4 %0, %1, %2, %3 offen nt" : "=v"(v) : "v"(voff), "s"(rs), "s"(soff) : "memory"); return v;
}
DI unsigned ld_def(__amdgpu_buffer_rsrc_t rs, unsigned voff, unsigned soff) {
  unsigned v; asm volatile("buffer_load_dword %0, %1, %2, %3 offen" : "=v"(v) : "v"(voff), "s"(rs), "s"(soff) : "memory"); return v;
}
DI u64 ld_sc1(const void* base, unsigned voff) {
  u64 v; asm volatile("global_load_dwordx2 %0, %1, %2 sc1" : "=v"(v) : "v"(voff), "s"(base) : "memory"); return v;
}
DI unsigned pk(float a, float b) {
  const bf16x2 v = __builtin_convertvector((f32x2){a, b}, bf16x2);
  return __builtin_bit_cast(unsigned, v);
}
DI u32x4 pack8(const f32x16& x, int s) {
  return (u32x4){pk(x[8 * s], x[8 * s + 1]), pk(x[8 * s + 2], x[8 * s + 3]), pk(x[8 * s + 4], x[8 * s + 5]), pk(x[8 * s + 6], x[8 * s + 7])};
}
DI bf16x8 rd128(unsigned a) { return *(const LAS bf16x8*)(uintptr_t)a; }
DI void wr128(unsigned a, u32x4 v) { *(LAS u32x4*)(uintptr_t)a = v; }
DI float rd32(unsigned a) { return *(const LAS float*)(uintptr_t)a; }
DI void wr32(unsigned a, float v) { *(LAS float*)(uintptr_t)a = v; }
DI s16x4 rdtr(unsigned a) { return __builtin_amdgcn_ds_read_tr16_b64_v4i16((LAS s16x4*)(uintptr_t)a); }
DI bf16x8 rdtr2(unsigned a, int step) {
  const s16x4 lo = rdtr(a), hi = rdtr(a + step);
  return __builtin_shufflevector(lo, hi, 0, 1, 2, 3, 4, 5, 6, 7);
}
DI float ex2(float v) { return __builtin_amdgcn_exp2f(v); }
DI float max3(float a, float b, float c) { return fmaxf(fmaxf(a, b), c); }
DI f32x2 xhalf(float v) {
  auto r = __builtin_amdgcn_permlane32_swap(__float_as_uint(v), __float_as_uint(v), false, false);
  return (f32x2){__uint_as_float(r[0]), __uint_as_float(r[1])};
}

constexpr int XBUF(int j) { return (j & 1) * 49152; }
constexpr int WBUF(int s) { return ((s >> 1) & 1) * 49152 + 16384 + (s & 1) * 16384; }
constexpr int KSLOT(int t) { const int m[8] = {4, 3, 4, 1, 2, 0, 3, 4}; return m[t] * SLOT; }
constexpr int VSLOT(int t) { const int m[4] = {1, 2, 0, 4}; return m[t & 3] * SLOT; }
constexpr int OSLOT(int j) { const int m[7] = {1, 2, 0, 3, 4, 1, 2}; return m[j] * SLOT; }
constexpr int QIMG = SLOT, PRING = 3 * SLOT, OIMG = 3 * SLOT;
constexpr int KORD(int i) { return (i >> 1) + (i & 1) * 8; }

enum { OP_X, OP_W, OP_K, OP_V, OP_O, OP_ST, OP_EXT  , OP_PF, OP_EP };
enum { IV_K = 15, IV_SM = 23, IV_V = 24, IV_O = 33 };
struct Op { int iv, pos, kind, idx, pc; };
struct Tab { Op op[192]; int n; };
#define QUOTA { 0, 2, 0, 2, 0, 2, 0, 2, 0, 2, 0, 3, 2, 2, 2,  3, 2, 4, 4, 3, 3, 3, 3, 2, 2, 3, 3, 3, 3, 4, 4, 8, 0}
#define NEARLY 4
constexpr int NE = NEARLY, LSPAN = 96;
constexpr int NLAND = 3;
constexpr int avail(int s) {
  return s < 5 ? 0 : s == 5 ? 14 : s < 11 ? IV_K + s - 4 : s == 11 ? IV_SM : IV_V + s - 11; }
constexpr Tab make_tab() {
  Tab t{}; int n = 0;
  auto add = [&](int iv, int pos, int kind, int idx, int pc) { t.op[n] = Op{iv, pos, kind, idx, pc}; ++n; };
  for (int i = 0; i < 2; ++i) add(-1, 0, OP_X, 0, i);
  for (int i = 0; i < 2; ++i) add(-1, 0, OP_W, 0, i);
  add(-1, 0, OP_EXT, 0, 0);
  for (int i = 0; i < 2; ++i) add(-1, 1, OP_W, 1, i);
  for (int i = 0; i < 2; ++i) add(-1, 1, OP_X, 1, i);
  for (int i = 0; i < 4; ++i) add(0, 4 + i, OP_W, 2 + (i >> 1), i & 1);
  const int quota[33] = QUOTA;
  int sp = 0;
  for (int iv = 1; iv <= 32; ++iv) {
    int p0 = 2, p1 = 16;
    if (iv <= 10) {
      if ((iv & 1) == 0) { add(iv, 1, OP_X, iv / 2 + 1, 0); add(iv, 2, OP_X, iv / 2 + 1, 1); add(iv, 3, OP_W, iv + 3, 0); add(iv, 5, OP_W, iv + 3, 1); p0 = 6; }
      else { add(iv, 2, OP_W, iv + 3, 0); add(iv, 3, OP_W, iv + 3, 1); p0 = 5; }
      p1 = 8;
    } else if (iv <= 13) p1 = 8;
    else if (iv == 14 || iv == 32) p1 = 4;
    else if (iv == IV_SM) { p0 = 0; p1 = 1; }
    else if (iv == IV_V) p0 = 6;
    if (iv == PFIV) add(iv, 1, OP_PF, 0, 0);
    if (iv == IV_V + 5) add(iv, 1, OP_EP, 0, 0);
    const int q = quota[iv];
    for (int i = 0; i < q && sp < 76 && avail(sp >> 2) <= iv; ++i, ++sp) {
      const int s = sp >> 2;
      add(iv, p0 + (i * (p1 - p0)) / q, s < 8 ? OP_K : s < 16 ? OP_V : OP_O, s & 7, sp & 3);
    }
  }
  for (int j = 1; j <= 6; ++j) {
    if (j <= 2) for (int i = 0; i < 8; ++i) add(IV_O + j, i, OP_O, 1 + 2 * j + (i >> 2), i & 3);
    add(IV_O + j, 9, OP_ST, j - 1, 0); add(IV_O + j, 13, OP_ST, j - 1, 1);
  }
  t.n = n; return t;
}
constexpr Tab TAB = make_tab();
constexpr int op_first(int iv, int pos) { int i = 0; while (i < TAB.n && (TAB.op[i].iv < iv || (TAB.op[i].iv == iv && TAB.op[i].pos < pos))) ++i; return i; }
constexpr int inflight(int iv, int pos, int kind, int idx) {
  int last = -1, cnt = 0; for (int i = 0; i < TAB.n; ++i) if (TAB.op[i].kind == kind && TAB.op[i].idx == idx) { last = i; ++cnt; }
  const int lim = op_first(iv, pos);
  return (cnt != ((kind == OP_X || kind == OP_W || kind == OP_ST) ? 2 : kind >= OP_EXT ? 1 : 4) || last >= lim) ? -1 : lim - 1 - last; }
constexpr int cmin(int a, int b) { return a < b ? a : b; }

__global__ __launch_bounds__(512, 2)
void attn_fused(const char* __restrict__ xA, const char* __restrict__ kA, const char* __restrict__ vA,
                const char* __restrict__ wqA, const char* __restrict__ woA,
                const char* __restrict__ xB, const char* __restrict__ kB, const char* __restrict__ vB,
                const char* __restrict__ wqB, const char* __restrict__ woB, char* ws, char* out) {
  __shared__ __attribute__((aligned(1024))) char smem[LDS_BYTES];
  const unsigned lds0 = (unsigned)(uintptr_t)(LAS char*)smem;

  const int xcd = blockIdx.x & 7, idx = blockIdx.x >> 3;
  const int br = xcd >> 2, head = (xcd & 3) * 4 + (idx & 3), bat = idx >> 2, grp = br * NB + bat;
  const int tid = threadIdx.x, lane = tid & 63;
  const int wid = __builtin_amdgcn_readfirstlane(tid >> 6);
  const int r = lane & 31, h = lane >> 5, g = (lane >> 4) & 1, q4 = (lane >> 2) & 3, p = lane & 3;
  const int wl = wid & 3, wh = wid >> 2;
  unsigned stv = 0; (void)stv; STAMP(0); STAMPC(62);

  const char* xg  = (br ? xB : xA) + (size_t)bat * SQ * DM * 2;
  const char* kg  = (br ? kB : kA) + (size_t)(bat * NH + head) * SK * DH * 2;
  const char* vg  = (br ? vB : vA) + (size_t)(bat * NH + head) * SK * DH * 2;
  const char* wqg = (br ? wqB : wqA) + (size_t)head * DH * 2;
  const char* wog = (br ? woB : woA) + (size_t)head * DH * DM * 2;
  const __amdgpu_buffer_rsrc_t rsX  = __builtin_amdgcn_make_buffer_rsrc((void*)xg, 0, SQ * DM * 2, 0x00020000);
  const __amdgpu_buffer_rsrc_t rsK  = __builtin_amdgcn_make_buffer_rsrc((void*)kg, 0, SK * DH * 2, 0x00020000);
  const __amdgpu_buffer_rsrc_t rsV  = __builtin_amdgcn_make_buffer_rsrc((void*)vg, 0, SK * DH * 2, 0x00020000);
  const __amdgpu_buffer_rsrc_t rsWq = __builtin_amdgcn_make_buffer_rsrc((void*)wqg, 0, (DM - 1) * NH * DH * 2 + DH * 2, 0x00020000);
  const __amdgpu_buffer_rsrc_t rsWo = __builtin_amdgcn_make_buffer_rsrc((void*)wog, 0, DH * DM * 2, 0x00020000);
  const __amdgpu_buffer_rsrc_t rsP  = __builtin_amdgcn_make_buffer_rsrc((void*)(ws + WSOFF + CTRL_BYTES + (size_t)grp * GRP_BYTES), 0, NH * PART_BYTES, 0x00020000);

  const unsigned swzT = (unsigned)((r ^ ((2 * (wid & 1) + h) << 2)) << 4);
  const unsigned voWq = (unsigned)h * (NH * DH * 2) + swzT;
  const unsigned voV  = (unsigned)h * 512u + swzT;
  const unsigned voK  = (unsigned)h * 512u + (unsigned)((r ^ h ^ ((2 * wid) & 15)) << 4);
  const unsigned voX  = (unsigned)(lane >> 3) * (DM * 2) + (unsigned)(((lane & 7) ^ (4 * (wid & 1) + (lane >> 4))) << 4);
  unsigned voWo = 0;

  const int rot1 = ROT1, rot4 = ROT4;
  auto rotc = [](int j, int rot) { const int c = j + rot; return c >= 7 ? c - 7 : c; };
  const unsigned ldw = lds0 + wid * 1024;
  const unsigned frag = lds0 + lane * 16;
  unsigned so = 0;
  u32x4 kland[NLAND * 4]; unsigned pfr = 0;
  f32x16 C[7];
  u32x4 ow[2];
  float inv = 0.f;
  unsigned pw = 0;
  u64 word = 0;

  auto issue = [&](auto IV, auto POS) {
    constexpr int f = op_first(IV.v, POS.v), c = op_first(IV.v, POS.v + 1) - f;
    sfor<c>([&](auto I) { constexpr Op o = TAB.op[f + I.v];
      if constexpr (o.kind == OP_X) {
        if constexpr (o.pc == 0) { so = (unsigned)(wid * 8 * DM * 2 + rotc(o.idx, rot1) * 128); PINS(so); }
        dma(rsX, voX, so + o.pc * (64 * DM * 2), ldw + XBUF(o.idx) + o.pc * 8192);
      } else if constexpr (o.kind == OP_W) {
        if constexpr (o.pc == 0) { so = (unsigned)((rotc(o.idx >> 1, rot1) * 64 + (o.idx & 1) * 32 + 2 * wid) * (NH * DH * 2)); PINS(so); }
        dma(rsWq, voWq, so + o.pc * 16 * (NH * DH * 2), ldw + WBUF(o.idx) + o.pc * 8192);
      } else if constexpr (o.kind == OP_K) {
        if constexpr (o.idx >= 2 && o.idx < 2 + NLAND) kland[(o.idx - 2) * 4 + o.pc] = ld_nt(rsK, voK, (unsigned)(wid * 1024 + o.idx * SLOT + o.pc * 8192));
        else dma_nt(rsK, voK, (unsigned)(wid * 1024 + o.idx * SLOT + o.pc * 8192), ldw + KSLOT(o.idx) + o.pc * 8192);
      } else if constexpr (o.kind == OP_V) {
        dma_nt(rsV, voV, (unsigned)(wid * 1024 + o.idx * SLOT + o.pc * 8192), ldw + VSLOT(o.idx) + o.pc * 8192);
      } else if constexpr (o.kind == OP_O) {
        if constexpr (o.pc == 0) { so = (unsigned)(wid * 8 * DM * 2 + rotc(o.idx, rot4) * 128); PINS(so); }
        dma_o(rsWo, voWo, so + o.pc * (64 * DM * 2), ldw + OSLOT(o.idx) + o.pc * 8192);
      } else if constexpr (o.kind == OP_PF) {
        pfr = ld_def(rsWo, lane < 28 ? (unsigned)lane * 128u : 0x40000000u, (unsigned)((bat * 8 + wid) * 28 * 128));
      } else if constexpr (o.kind == OP_EP) {
        word = ld_sc1(ws + grp * 256, 0u);
      } else if constexpr (o.kind == OP_ST) {
        __builtin_amdgcn_raw_buffer_store_b128(ow[o.pc], rsP, pw + o.idx * 16384 + o.pc * 1024, 0, STAUX  );
      } }); };
  auto park = [&](auto T, auto I) { wr128(frag + wid * 1024 + KSLOT(T.v) + I.v * 8192, kland[(T.v - 2) * 4 + I.v]); };

  issue(IC<-1>{}, IC<0>{});
  {
    u64 pcv; unsigned len;
    asm volatile("s_getpc_b64 %0\n\t.Lattn_code_begin:\n\ts_mov_b32 %1, .Lattn_code_end - .Lattn_code_begin" : "=s"(pcv), "=s"(len));
    const __amdgpu_buffer_rsrc_t rsC = __builtin_amdgcn_make_buffer_rsrc((void*)(pcv & ~127ull), 0, len & ~15u, 0x00020000);
    dma(rsC, frag - lds0, (unsigned)((wid * 32 + idx) * 1024), ldw + KSLOT(0));
  }
  lds_bar();
  issue(IC<-1>{}, IC<1>{});

  const unsigned trA = lds0 + (8 * h + q4) * 512 + (((8 * wl + 2 * g + (p & 1)) ^ (q4 << 2)) << 4) + (p >> 1) * 8;

  f32x16 acc[2][2];
  sfor<4>([&](auto i) { acc[i.v >> 1][i.v & 1] = f32x16{}; });
  unsigned tb = 0;
  auto putT = [&](int base, auto I) { constexpr int dt = I.v >> 1, qt = I.v & 1;
    if constexpr (I.v == 0) { tb = frag + base + (16 * wl + 2 * wh) * 1024; PIN(tb); }
    sfor<2>([&](auto s) { wr128(tb + ((2 * dt + s.v) * 4 + qt) * 1024, pack8(acc[dt][qt], s.v)); }); };
  {
    const unsigned xrd = lds0 + (64 * wh + r) * 128 + ((h ^ ((r >> 1) & 7)) << 4);
    bf16x8 a0[29], a1[29], b0[29], b1[29];
    auto rdA = [&](auto U) { constexpr int u = U.v, s = u >> 1, ks = u & 1, kk = u & 3;
      a0[u] = rdtr2(trA + WBUF(s) + ks * 8192, 2048); b0[u] = rd128((xrd ^ (kk << 5)) + XBUF(s >> 1)); };
    auto rdB = [&](auto U) { constexpr int u = U.v, s = u >> 1, ks = u & 1, kk = u & 3;
      b1[u] = rd128((xrd ^ (kk << 5)) + XBUF(s >> 1) + 4096); a1[u] = rdtr2((trA ^ 64) + WBUF(s) + ks * 8192, 2048); };
    wait_bar<cmin(inflight(0, 0, OP_X, 0), inflight(0, 0, OP_W, 0))>(); STAMP(1);
    rdA(IC<0>{}); rdB(IC<0>{}); SBAR();
    sfor<28>([&](auto U) { constexpr int u = U.v;
      constexpr int sn = (u + 1) >> 1, k0 = ((u + 1) & 1) * 4;
      if constexpr ((u & 1) == 1) {
        if constexpr (sn < 14) wait_bar<cmin(inflight(sn, 0, OP_X, sn >> 1), inflight(sn, 0, OP_W, sn < 14 ? sn : 0))>();
        else lds_bar();
        STAMP(1 + sn);
      }
      PRIO(3 - k0 / 2);
      acc[0][0] = MFMA(a0[u], b0[u], acc[0][0]); SBAR();
      if constexpr (u + 1 < 28) rdA(IC<u + 1>{});
      issue(IC<sn>{}, IC<k0>{}); SBAR();
      acc[0][1] = MFMA(a0[u], b1[u], acc[0][1]); SBAR();
      if constexpr (u + 1 < 28) rdB(IC<u + 1>{});
      issue(IC<sn>{}, IC<k0 + 1>{}); SBAR();
      PRIO(2 - k0 / 2);
      acc[1][0] = MFMA(a1[u], b0[u], acc[1][0]); SBAR(); issue(IC<sn>{}, IC<k0 + 2>{});
      if constexpr (u == 27) putT(QIMG, IC<0>{});
      SBAR();
      acc[1][1] = MFMA(a1[u], b1[u], acc[1][1]); SBAR(); issue(IC<sn>{}, IC<k0 + 3>{}); SBAR();
    });
  }
  putT(QIMG, IC<1>{}); putT(QIMG, IC<2>{}); putT(QIMG, IC<3>{});

  bf16x8 qf[16];
  f32x16 S[8];
  float mh[NE], lt[NE + 1], mx = -__builtin_inff(), ma = 0.f, nm = 0.f;
  auto tmax = [&](auto T, auto K) { constexpr int t = T.v;
    if constexpr (K.v == 0) { ma = max3(max3(S[t][0], S[t][1], S[t][2]), S[t][3], S[t][4]); ma = max3(ma, S[t][5], S[t][6]); mx = max3(mx, S[t][7], S[t][8]); PIN(ma); PIN(mx); }
    else { ma = max3(ma, S[t][9], S[t][10]); mx = max3(mx, S[t][11], S[t][12]); ma = max3(ma, S[t][13], S[t][14]); mx = max3(mx, S[t][15], ma); PIN(mx); } };
  auto estep = [&](auto N, auto LO, auto HI) { constexpr int n = N.v, lo = LO.v, hi = HI.v;
    if constexpr (n < hi) { constexpr int t = (n >> 4) & 7, e = n & 15;
      if constexpr (e == 0 && t < NE) { mh[t < NE ? t : 0] = mx * SCL; nm = -mh[t < NE ? t : 0]; }
      float x = fmaf(S[t][e], SCL, nm); PIN(x); S[t][e] = x; }
    if constexpr (n - 1 >= lo && n - 1 < hi) { constexpr int t = ((n - 1) >> 4) & 7, e = (n - 1) & 15; float x = ex2(S[t][e]); PIN(x); S[t][e] = x; }
    if constexpr (n - 2 >= lo && n - 2 < hi) { constexpr int t = ((n - 2) >> 4) & 7, e = (n - 2) & 15, a = t < NE ? t : NE;
      if constexpr (e == 0 && t <= NE) lt[a] = S[t][e]; else { lt[a] += S[t][e]; PIN(lt[a]); } } };
  {
    const int kr = (r & 19) | ((r & 4) << 1) | ((r & 8) >> 1);
    const unsigned krd = lds0 + (32 * wh + kr) * 512 + ((h ^ (kr & 15)) << 4);
    bf16x8 af[128];
    unsigned kb = 0;
    auto rd = [&](auto T, auto I) { constexpr int t = T.v, ks = KORD(I.v);
      if constexpr (t == 0) { if constexpr (I.v == 0) { tb = frag + QIMG + wl * 1024; PIN(tb); } qf[ks] = rd128(tb + ks * 4096); }
      af[16 * t + I.v] = rd128((kb ^ ((ks & 7) << 5)) + (ks >> 3) * 256); };
    auto mm = [&](auto T, auto I) { constexpr int t = T.v, i = I.v;
      if constexpr (i == 0) S[t] = MFMA(af[16 * t], qf[KORD(0)], f32x16{}); else S[t] = MFMA(af[16 * t + i], qf[KORD(i)], S[t]); };
    sfor<8>([&](auto T) { constexpr int t = T.v;
      wait_bar<cmin(63, cmin(inflight(IV_K + t, 0, OP_K, t), inflight(IV_K + t, 0, OP_K, (t >= 1 && t <= 3) ? t + 1 : t)))>(); STAMP(16 + t);
      kb = krd + KSLOT(t); PIN(kb);
      if constexpr (IV_K + t == PFIV + 2) { wait_vm<PFIV < 0 ? 0 : inflight(IV_K + t, 0, OP_PF, 0)>(); PIN(pfr); }
      sfor<16>([&](auto K) { constexpr int k = K.v;
        if constexpr ((k & 3) == 0) PRIO(3 - (k >> 2));
        if constexpr (k < 3) { if constexpr (t > 0) mm(IC<(t > 0 ? t - 1 : 0)>{}, IC<13 + k>{}); }
        else mm(T, IC<k - 3>{});
        SBAR();
        if constexpr (k < 2) { rd(T, IC<2 * k>{}); rd(T, IC<2 * k + 1>{}); }
        else if constexpr (k < 14) rd(T, IC<k + 2>{});
        issue(IC<IV_K + t>{}, K);
        if constexpr (t >= 1 && t <= 3 && k >= 3 && k <= 12 && k % 3 == 0) park(IC<(t >= 1 && t <= 3) ? t + 1 : 2>{}, IC<(k / 3 - 1) & 3>{});
        if constexpr (t > 0 && (k == 5 || k == 6)) tmax(IC<(t > 0 ? t - 1 : 0)>{}, IC<(k - 5) & 1>{});
        if constexpr ((t > 1 || (t == 1 && k >= 7)) && k != 5 && k != 6) {
          constexpr int y = (t - 1) * 14 + (k < 5 ? k : k - 2) - 5, span = 7 * 14 - 5, tot = NE * 16 + 2, n0 = y * tot / span, n1 = (y + 1) * tot / span;
          sfor<n1 - n0>([&](auto i) { static_assert(((n0 + i.v) >> 4) < t || n0 + i.v >= NE * 16, "scores not ready");
            static_assert(((n0 + i.v) >> 4) != t - 1 || k >= 7 || n0 + i.v >= NE * 16, "max not taken yet");
            estep(IC<n0 + i.v>{}, IC<0>{}, IC<NE * 16>{}); }); }
        SBAR();
      });
    });
    mm(IC<7>{}, IC<13>{}); mm(IC<7>{}, IC<14>{}); mm(IC<7>{}, IC<15>{});
    STAMP(24);
  }
  tmax(IC<7>{}, IC<0>{}); tmax(IC<7>{}, IC<1>{});
  {
    const f32x2 m2 = xhalf(mx * SCL);
    const float mw = fmaxf(m2[0], m2[1]);
    if (h == 0) wr32(lds0 + PRING + ((2 * (1 - wh)) * 4 + wl) * 1024 + r * 4, mw);
    lds_bar(); STAMP(25);
    nm = -fmaxf(mw, rd32(lds0 + PRING + ((2 * wh) * 4 + wl) * 1024 + r * 4));
  }
  float fs = 1.f, ls = 0.f; u32x4 pv;
  unsigned pwr = frag + PRING + (8 * wh + wl) * 1024, prd = frag + PRING + 2 * wh * 1024; PIN(pwr); PIN(prd);
  auto putP = [&](auto T, auto Q) { constexpr int t = T.v, q = Q.v, e = 4 * q;
    if constexpr (q == 0 && t < NE) { fs = ex2(mh[t < NE ? t : 0] + nm); if constexpr (t == 0) ls = lt[0] * fs; else ls = fmaf(lt[t < NE ? t : 0], fs, ls); }
    if constexpr (t < NE) { pv[2 * (q & 1)] = pk(S[t][e] * fs, S[t][e + 1] * fs); pv[2 * (q & 1) + 1] = pk(S[t][e + 2] * fs, S[t][e + 3] * fs); }
    else { pv[2 * (q & 1)] = pk(S[t][e], S[t][e + 1]); pv[2 * (q & 1) + 1] = pk(S[t][e + 2], S[t][e + 3]); }
    if constexpr (q & 1) wr128(pwr + (t & 1) * 16384 + (q >> 1) * 4096, pv); else PIN(pv); };
  sfor<4>([&](auto q) { putP(IC<0>{}, q); });
  issue(IC<IV_SM>{}, IC<0>{});
  voWo = (unsigned)(lane >> 3) * (DM * 2) + (unsigned)(((lane & 7) ^ (g << 2)) << 4);

  sfor<4>([&](auto i) { acc[i.v >> 1][i.v & 1] = f32x16{}; });
  {
    bf16x8 a0[33], a1[33], b0[33], b1[33];
    unsigned vb = 0;
    auto rdA = [&](auto U) { constexpr int u = U.v, t = u >> 2, ks = u & 3;
      a0[u] = rdtr2(vb + ks * 8192, 2048); b0[u] = rd128(prd + (t & 1) * 16384 + ks * 4096); };
    auto rdB = [&](auto U) { constexpr int u = U.v, t = u >> 2, ks = u & 3;
      b1[u] = rd128(prd + (t & 1) * 16384 + ks * 4096 + 1024); a1[u] = rdtr2((vb ^ 64) + ks * 8192, 2048); };
    wait_bar<inflight(IV_V, 0, OP_V, 0)>(); STAMP(26);
    vb = trA + VSLOT(0); PIN(vb);
    rdA(IC<0>{}); rdB(IC<0>{}); SBAR();
    sfor<32>([&](auto U) { constexpr int u = U.v;
      constexpr int tn = (u + 1) >> 2, gi = (u + 1) & 3;
      if constexpr (gi == 0) {
        if constexpr (tn < 8) { wait_bar<inflight(IV_V + tn, 0, OP_V, tn < 8 ? tn : 0)>(); vb = trA + VSLOT(tn); PIN(vb); } else lds_bar();
        STAMP(26 + tn);
      }
      auto side = [&](auto Z) { constexpr int z = 2 * gi + Z.v;
        if constexpr (tn < 7 && z >= 2 && z < 6) putP(IC<(tn < 7 ? tn + 1 : 0)>{}, IC<(z - 2) & 3>{});
        if constexpr (tn == 7 && z == 0) { const f32x2 l2 = xhalf(ls + lt[NE]); ls = l2[0] + l2[1]; if (h == 0) wr32(lds0 + PRING + wid * 128 + r * 4, ls); }
        if constexpr (tn == 7 && z == 3) lds_bar();
        if constexpr (tn == 7 && z == 4) { inv = 1.0f / (ls + rd32(lds0 + PRING + (wid ^ 4) * 128 + r * 4)); PIN(inv); }
        if constexpr (u == 31 && Z.v == 0) putT(OIMG, IC<0>{}); };
      auto late = [&](auto Y) { constexpr int y = 4 * u + Y.v, tot = (8 - NE) * 16 + 2, e0 = y < LSPAN ? y * tot / LSPAN : tot, e1 = y < LSPAN ? (y + 1) * tot / LSPAN : tot;
        sfor<e1 - e0>([&](auto i) { constexpr int n = NE * 16 + e0 + i.v; static_assert(n < NE * 16 + 2 || y < 16 * (((n - 2) >> 4) - 1) + 2, "P tile needed earlier");
          estep(IC<n>{}, IC<NE * 16>{}, IC<128>{}); }); };
      PRIO(3 - gi);
      acc[0][0] = MFMA(a0[u], b0[u], acc[0][0]); SBAR();
      if constexpr (u + 1 < 32) rdA(IC<u + 1>{});
      late(IC<0>{});
      issue(IC<IV_V + tn>{}, IC<4 * gi>{}); SBAR();
      acc[0][1] = MFMA(a0[u], b1[u], acc[0][1]); SBAR();
      if constexpr (u + 1 < 32) rdB(IC<u + 1>{});
      late(IC<1>{});
      issue(IC<IV_V + tn>{}, IC<4 * gi + 1>{}); SBAR();
      acc[1][0] = MFMA(a1[u], b0[u], acc[1][0]); SBAR();
      issue(IC<IV_V + tn>{}, IC<4 * gi + 2>{}); late(IC<2>{}); side(IC<0>{}); SBAR();
      acc[1][1] = MFMA(a1[u], b1[u], acc[1][1]); SBAR();
      issue(IC<IV_V + tn>{}, IC<4 * gi + 3>{}); late(IC<3>{}); side(IC<1>{}); SBAR();
    });
  }
  putT(OIMG, IC<1>{}); putT(OIMG, IC<2>{}); putT(OIMG, IC<3>{});

  GAS u64* ctrl = (GAS u64*)(ws + grp * 256);
  u64 tag = 0;
  u32x4 v[NH];
  unsigned done = 0;
  auto gather = [&](u64 f) {
    const unsigned fresh = (((unsigned)__ballot(f == tag) & 0xffffu) | (1u << head)) & ~done;
    __builtin_amdgcn_fence(__ATOMIC_ACQUIRE, "wavefront");
    if (fresh) {
      const unsigned off = (unsigned)((wid * 16 + head) * 1024) + (frag - lds0);
      sfor<NH>([&](auto i) { if (fresh & (1u << i.v)) v[i.v] = __builtin_amdgcn_raw_buffer_load_b128(rsP, off, i.v * PART_BYTES, LDAUX); });
      done |= fresh;
    } };

  {
    const unsigned trW = lds0 + (8 * h + q4) * 128 + (((4 * wh + 2 * g + (p & 1)) ^ ((q4 >> 1) << 2)) << 4) + (p >> 1) * 8;
    pw = (unsigned)(head * PART_BYTES + wid * 2048) + (frag - lds0);
    bf16x8 af[112];
    unsigned wb = 0;
    auto rd = [&](auto J, auto I) { constexpr int j = J.v, ks = I.v;
      if constexpr (j == 0) { if constexpr (ks == 0) { tb = frag + OIMG + wl * 1024; PIN(tb); } qf[ks] = rd128(tb + ks * 4096); }
      af[16 * j + ks] = rdtr2(wb + ks * 2048, 512); };
    auto mm = [&](auto J, auto I) { constexpr int j = J.v, i = I.v;
      if constexpr (i == 0) C[j] = MFMA(af[16 * j], qf[0], f32x16{}); else C[j] = MFMA(af[16 * j + i], qf[i], C[j]); };
    sfor<7>([&](auto J) { constexpr int j = J.v;
      wait_bar<cmin(inflight(IV_O + j, 0, OP_O, j), j == 0 ? inflight(IV_O, 0, OP_EP, 0) : 63)>();
      STAMP(35 + j);
      if constexpr (j == 0) {
        PIN(word);
        const u64 e = ((u64)(unsigned)__builtin_amdgcn_readfirstlane((unsigned)(word >> 32)) << 32) | (unsigned)__builtin_amdgcn_readfirstlane((unsigned)word);
        tag = e * 0x9E3779B97F4A7C15ull + 0x632BE59BD9B4E019ull;
      }
      wb = trW + OSLOT(j); PIN(wb);
      sfor<16>([&](auto K) { constexpr int k = K.v;
        if constexpr ((k & 3) == 0) PRIO(3 - (k >> 2));
        if constexpr (k < 3) { if constexpr (j > 0) mm(IC<(j > 0 ? j - 1 : 0)>{}, IC<13 + k>{}); }
        else mm(J, IC<k - 3>{});
        SBAR();
        if constexpr (k < 2) { rd(J, IC<2 * k>{}); rd(J, IC<2 * k + 1>{}); }
        else if constexpr (k < 14) rd(J, IC<k + 2>{});
        issue(IC<IV_O + j>{}, K);
        if constexpr (j > 0 && k >= 5 && k < 13) { constexpr int e = k - 5, jp = j > 0 ? j - 1 : 0;
          unsigned w = pk(C[jp][2 * e] * inv, C[jp][2 * e + 1] * inv); PIN(w); ow[e >> 2][e & 3] = w; }
        SBAR();
      });
    });
    mm(IC<6>{}, IC<13>{}); mm(IC<6>{}, IC<14>{}); mm(IC<6>{}, IC<15>{});
    sfor<16>([&](auto e) { C[6][e.v] *= inv; });
    sfor<2>([&](auto s) { __builtin_amdgcn_raw_buffer_store_b128(pack8(C[6], s.v), rsP, pw + 6 * 16384 + s.v * 1024, 0, STAUX); });
  }
  STAMP(42);

  word = ld_sc1(ws + grp * 256 + 128, (frag - lds0) >> 1 & 0x78u);
  wait_bar<0>();
  PIN(word);
  STAMP(43);
  if (tid == 448) __hip_atomic_store(ctrl + 16 + head, tag, RLX_AGENT);
  if (wid < 7) {
    gather(word);
    for (unsigned spins = 0; spins < (1u << 18) && done != 0xffffu; ++spins) {
      gather(__hip_atomic_load(ctrl + 16 + (lane & 15), RLX_AGENT));
      if (done != 0xffffu) __builtin_amdgcn_s_sleep(2);
    }
    STAMP(44);
    if (wid == 6 && lane == 0 && head == 0) __hip_atomic_store(ctrl, tag, RLX_AGENT);
    float s[8];
    sfor<8>([&](auto i) { s[i.v] = 0.f; });
    sfor<NH>([&](auto hh) { sfor<4>([&](auto i) {
      s[2 * i.v] += __uint_as_float(v[hh.v][i.v] << 16); s[2 * i.v + 1] += __uint_as_float(v[hh.v][i.v] & 0xffff0000u); }); });
    const int q = 32 * ((head >> 1) & 3) + r, n = 64 * rotc(wid, rot4) + 32 * (head >> 3) + 16 * (head & 1) + 8 * h;
    *(u32x4*)(out + ((size_t)(bat * SQ + q) * (2 * DM) + br * DM + n) * 2) =
        (u32x4){pk(s[0], s[1]), pk(s[2], s[3]), pk(s[4], s[5]), pk(s[6], s[7])};
    STAMP(45);
    asm volatile(".Lattn_code_end:");
  }
}

extern "C" void kernel_launch(void* const* d_in, const int* in_sizes, int n_in,
                              void* d_out, int out_size, void* d_ws, size_t ws_size,
                              hipStream_t stream) {
  attn_fused<<<256, 512, 0, stream>>>((const char*)d_in[0], (const char*)d_in[1], (const char*)d_in[2], (const char*)d_in[3], (const char*)d_in[4],
                                      (const char*)d_in[5], (const char*)d_in[6], (const char*)d_in[7], (const char*)d_in[8], (const char*)d_in[9],
                                      (char*)d_ws, (char*)d_out);
}
```

```cpp
#include <hip/hip_runtime.h>
#include <hip/hip_bf16.h>
#include <stdint.h>

typedef short    bf16x8 __attribute__((ext_vector_type(8)));
typedef short    s16x4  __attribute__((ext_vector_type(4)));
typedef float    f32x16 __attribute__((ext_vector_type(16)));
typedef float    f32x2  __attribute__((ext_vector_type(2)));
typedef __bf16   bf16x2 __attribute__((ext_vector_type(2)));
typedef unsigned u32x4  __attribute__((ext_vector_type(4)));
typedef unsigned long long u64;
#define LAS __attribute__((address_space(3)))
#define GAS __attribute__((address_space(1)))

constexpr int NB = 8, SQ = 128, DM = 448, NH = 16, SK = 512, DH = 256;
constexpr int SLOT = 32768;
constexpr int LDS_BYTES = 5 * SLOT;
constexpr int PART_BYTES = SQ * DM * 2;
constexpr int GRP_BYTES = NH * PART_BYTES + 4096;
constexpr int CTRL_BYTES = 16 * 256 + 4096;
constexpr float SCL = 0.0625f * 1.4426950408889634f;

#define STAMP(i) do {} while (0)
#define STAMPC(i) do {} while (0)
#define ROT1 ((bat + 2 * (head & 3)) % 7)
#define ROT4 (bat % 7)
#define OPOL ""
#define STAUX 16
#define LDAUX 16
#define WSOFF 0
#define PFIV -9
#define OUTAUX 0
#define WPOL ""

#define DI __device__ __forceinline__
#define MFMA(a, b, c) __builtin_amdgcn_mfma_f32_32x32x16_bf16((a), (b), (c), 0, 0, 0)
#define SBAR() __builtin_amdgcn_sched_barrier(0)
#define PIN(x) asm volatile("" : "+v"(x))
#define PRIO(n) __builtin_amdgcn_s_setprio(n)
#define PINS(x) asm volatile("" : "+s"(x))
#define RLX_AGENT __ATOMIC_RELAXED, __HIP_MEMORY_SCOPE_AGENT

template <int I> struct IC { static constexpr int v = I; };
template <int I, int N, class F> DI void sfor_(F& f) { if constexpr (I < N) { f(IC<I>{}); sfor_<I + 1, N>(f); } }
template <int N, class F> DI void sfor(F f) { sfor_<0, N>(f); }

template <int N> DI void wait_bar() { static_assert(N >= 0 && N < 64, "vmcnt"); SBAR(); asm volatile("s_waitcnt vmcnt(%0) lgkmcnt(0)\n\ts_barrier" :: "i"(N) : "memory"); SBAR(); }
template <int N> DI void wait_vm()  { static_assert(N >= 0 && N < 64, "vmcnt"); asm volatile("s_waitcnt vmcnt(%0)" :: "i"(N) : "memory"); }
DI void lds_bar() { SBAR(); asm volatile("s_waitcnt lgkmcnt(0)\n\ts_barrier" ::: "memory"); SBAR(); }

DI void dma(__amdgpu_buffer_rsrc_t rs, unsigned voff, unsigned soff, unsigned ldsb) {
  asm volatile("s_mov_b32 m0, %0\n\ts_nop 0\n\tbuffer_load_dwordx4 %1, %2, %3 offen " WPOL " lds"
               :: "s"(ldsb), "v"(voff), "s"(rs), "s"(soff) : "memory");
}
DI void dma_o(__amdgpu_buffer_rsrc_t rs, unsigned voff, unsigned soff, unsigned ldsb) {
  asm volatile("s_mov_b32 m0, %0\n\ts_nop 0\n\tbuffer_load_dwordx4 %1, %2, %3 offen " OPOL " lds"
               :: "s"(ldsb), "v"(voff), "s"(rs), "s"(soff) : "memory");
}
DI void dma_nt(__amdgpu_buffer_rsrc_t rs, unsigned voff, unsigned soff, unsigned ldsb) {
  asm volatile("s_mov_b32 m0, %0\n\ts_nop 0\n\tbuffer_load_dwordx4 %1, %2, %3 offen nt lds"
               :: "s"(ldsb), "v"(voff), "s"(rs), "s"(soff) : "memory");
}
DI u32x4 ld_nt(__amdgpu_buffer_rsrc_t rs, unsigned voff, unsigned soff) {
  u32x4 v; asm volatile("buffer_load_dwordx4 %0, %1, %2, %3 offen nt" : "=v"(v) : "v"(voff), "s"(rs), "s"(soff) : "memory"); return v;
}
DI unsigned ld_def(__amdgpu_buffer_rsrc_t rs, unsigned voff, unsigned soff) {
  unsigned v; asm volatile("buffer_load_dword %0, %1, %2, %3 offen" : "=v"(v) : "v"(voff), "s"(rs), "s"(soff) : "memory"); return v;
}
DI u64 ld_sc1(const void* base, unsigned voff) {
  u64 v; asm volatile("global_load_dwordx2 %0, %1, %2 sc1" : "=v"(v) : "v"(voff), "s"(base) : "memory"); return v;
}
DI void gather16(u32x4 (&v)[16], __amdgpu_buffer_rsrc_t rs, unsigned off, unsigned ra) {
  static_assert(PART_BYTES == 114688, "offsets below");
  unsigned done, cnt, f, so, t;
  asm volatile(
      "s_mov_b32 %[done], 0\n\ts_mov_b32 %[cnt], 0x400000\n"
      ".Lgl_%=:\n\tds_read_b32 %[t], %[ra]\n\ts_waitcnt lgkmcnt(0)\n\tv_readfirstlane_b32 %[f], %[t]\n\t"
      "s_andn2_b32 %[f], %[f], %[done]\n\ts_cbranch_scc0 .Lgi_%=\n\t"
      "s_bitcmp1_b32 %[f], 0\n\ts_cbranch_scc0 .Lg0_%=\n\ts_mov_b32 %[so], 0\n\tbuffer_load_dwordx4 %0, %[off], %[rs], %[so] offen sc1\n.Lg0_%=:\n\t"
      "s_bitcmp1_b32 %[f], 1\n\ts_cbranch_scc0 .Lg1_%=\n\ts_mov_b32 %[so], 114688\n\tbuffer_load_dwordx4 %1, %[off], %[rs], %[so] offen sc1\n.Lg1_%=:\n\t"
      "s_bitcmp1_b32 %[f], 2\n\ts_cbranch_scc0 .Lg2_%=\n\ts_mov_b32 %[so], 229376\n\tbuffer_load_dwordx4 %2, %[off], %[rs], %[so] offen sc1\n.Lg2_%=:\n\t"
      "s_bitcmp1_b32 %[f], 3\n\ts_cbranch_scc0 .Lg3_%=\n\ts_mov_b32 %[so], 344064\n\tbuffer_load_dwordx4 %3, %[off], %[rs], %[so] offen sc1\n.Lg3_%=:\n\t"
      "s_bitcmp1_b32 %[f], 4\n\ts_cbranch_scc0 .Lg4_%=\n\ts_mov_b32 %[so], 458752\n\tbuffer_load_dwordx4 %4, %[off], %[rs], %[so] offen sc1\n.Lg4_%=:\n\t"
      "s_bitcmp1_b32 %[f], 5\n\ts_cbranch_scc0 .Lg5_%=\n\ts_mov_b32 %[so], 573440\n\tbuffer_load_dwordx4 %5, %[off], %[rs], %[so] offen sc1\n.Lg5_%=:\n\t"
      "s_bitcmp1_b32 %[f], 6\n\ts_cbranch_scc0 .Lg6_%=\n\ts_mov_b32 %[so], 688128\n\tbuffer_load_dwordx4 %6, %[off], %[rs], %[so] offen sc1\n.Lg6_%=:\n\t"
      "s_bitcmp1_b32 %[f], 7\n\ts_cbranch_scc0 .Lg7_%=\n\ts_mov_b32 %[so], 802816\n\tbuffer_load_dwordx4 %7, %[off], %[rs], %[so] offen sc1\n.Lg7_%=:\n\t"
      "s_bitcmp1_b32 %[f], 8\n\ts_cbranch_scc0 .Lg8_%=\n\ts_mov_b32 %[so], 917504\n\tbuffer_load_dwordx4 %8, %[off], %[rs], %[so] offen sc1\n.Lg8_%=:\n\t"
      "s_bitcmp1_b32 %[f], 9\n\ts_cbranch_scc0 .Lg9_%=\n\ts_mov_b32 %[so], 1032192\n\tbuffer_load_dwordx4 %9, %[off], %[rs], %[so] offen sc1\n.Lg9_%=:\n\t"
      "s_bitcmp1_b32 %[f], 10\n\ts_cbranch_scc0 .Lg10_%=\n\ts_mov_b32 %[so], 1146880\n\tbuffer_load_dwordx4 %10, %[off], %[rs], %[so] offen sc1\n.Lg10_%=:\n\t"
      "s_bitcmp1_b32 %[f], 11\n\ts_cbranch_scc0 .Lg11_%=\n\ts_mov_b32 %[so], 1261568\n\tbuffer_load_dwordx4 %11, %[off], %[rs], %[so] offen sc1\n.Lg11_%=:\n\t"
      "s_bitcmp1_b32 %[f], 12\n\ts_cbranch_scc0 .Lg12_%=\n\ts_mov_b32 %[so], 1376256\n\tbuffer_load_dwordx4 %12, %[off], %[rs], %[so] offen sc1\n.Lg12_%=:\n\t"
      "s_bitcmp1_b32 %[f], 13\n\ts_cbranch_scc0 .Lg13_%=\n\ts_mov_b32 %[so], 1490944\n\tbuffer_load_dwordx4 %13, %[off], %[rs], %[so] offen sc1\n.Lg13_%=:\n\t"
      "s_bitcmp1_b32 %[f], 14\n\ts_cbranch_scc0 .Lg14_%=\n\ts_mov_b32 %[so], 1605632\n\tbuffer_load_dwordx4 %14, %[off], %[rs], %[so] offen sc1\n.Lg14_%=:\n\t"
      "s_bitcmp1_b32 %[f], 15\n\ts_cbranch_scc0 .Lg15_%=\n\ts_mov_b32 %[so], 1720320\n\tbuffer_load_dwordx4 %15, %[off], %[rs], %[so] offen sc1\n.Lg15_%=:\n\t"
      "s_or_b32 %[done], %[done], %[f]\n\ts_cmpk_eq_u32 %[done], 0xffff\n\ts_cbranch_scc1 .Lge_%=\n"
      ".Lgi_%=:\n\ts_sleep 1\n\ts_sub_u32 %[cnt], %[cnt], 1\n\ts_cbranch_scc0 .Lgl_%=\n"
      ".Lge_%=:\n\ts_waitcnt vmcnt(0)"
      : "=&v"(v[0]), "=&v"(v[1]), "=&v"(v[2]), "=&v"(v[3]), "=&v"(v[4]), "=&v"(v[5]), "=&v"(v[6]), "=&v"(v[7]), "=&v"(v[8]), "=&v"(v[9]), "=&v"(v[10]), "=&v"(v[11]), "=&v"(v[12]), "=&v"(v[13]), "=&v"(v[14]), "=&v"(v[15]),
        [done] "=&s"(done), [cnt] "=&s"(cnt), [f] "=&s"(f), [so] "=&s"(so), [t] "=&v"(t)
      : [ra] "v"(ra), [off] "v"(off), [rs] "s"(rs) : "memory", "scc");
}
DI unsigned pk(float a, float b) {
  const bf16x2 v = __builtin_convertvector((f32x2){a, b}, bf16x2);
  return __builtin_bit_cast(unsigned, v);
}
DI u32x4 pack8(const f32x16& x, int s) {
  return (u32x4){pk(x[8 * s], x[8 * s + 1]), pk(x[8 * s + 2], x[8 * s + 3]), pk(x[8 * s + 4], x[8 * s + 5]), pk(x[8 * s + 6], x[8 * s + 7])};
}
DI bf16x8 rd128(unsigned a) { return *(const LAS bf16x8*)(uintptr_t)a; }
DI void wr128(unsigned a, u32x4 v) { *(LAS u32x4*)(uintptr_t)a = v; }
DI float rd32(unsigned a) { return *(const LAS float*)(uintptr_t)a; }
DI void wr32(unsigned a, float v) { *(LAS float*)(uintptr_t)a = v; }
DI s16x4 rdtr(unsigned a) { return __builtin_amdgcn_ds_read_tr16_b64_v4i16((LAS s16x4*)(uintptr_t)a); }
DI bf16x8 rdtr2(unsigned a, int step) {
  const s16x4 lo = rdtr(a), hi = rdtr(a + step);
  return __builtin_shufflevector(lo, hi, 0, 1, 2, 3, 4, 5, 6, 7);
}
DI float ex2(float v) { return __builtin_amdgcn_exp2f(v); }
DI float max3(float a, float b, float c) { return fmaxf(fmaxf(a, b), c); }
DI f32x2 xhalf(float v) {
  auto r = __builtin_amdgcn_permlane32_swap(__float_as_uint(v), __float_as_uint(v), false, false);
  return (f32x2){__uint_as_float(r[0]), __uint_as_float(r[1])};
}

constexpr int XBUF(int j) { return (j & 1) * 49152; }
constexpr int WBUF(int s) { return ((s >> 1) & 1) * 49152 + 16384 + (s & 1) * 16384; }
constexpr int KSLOT(int t) { const int m[8] = {4, 3, 4, 3, 4, 3, 0, 1}; return m[t] * SLOT; }
constexpr int VSLOT(int t) { const int m[4] = {2, 4, 3, 1}; return m[t & 3] * SLOT; }
constexpr int OSLOT(int j) { const int m[7] = {2, 4, 3, 0, 1, 2, 4}; return m[j] * SLOT; }
constexpr int QIMG = SLOT, PRING = 0, OIMG = 0;
constexpr int KORD(int i) { return (i >> 1) + (i & 1) * 8; }

enum { OP_X, OP_W, OP_K, OP_V, OP_O, OP_ST, OP_EXT  , OP_PF, OP_EP };
enum { IV_K = 15, IV_SM = 23, IV_V = 24, IV_O = 33 };
struct Op { int iv, pos, kind, idx, pc; };
struct Tab { Op op[192]; int n; };
#define QUOTA { 0, 2, 0, 2, 0, 2, 0, 2, 0, 2, 0, 3, 2, 2, 2,  3, 3, 3, 3, 3, 3, 3, 3, 2, 3, 3, 3, 3, 3, 4, 4, 8, 0}
#define NEARLY 4
constexpr int NE = NEARLY, LSPAN = 96;
constexpr int NLAND = 4;
constexpr int avail(int s) {
  return s < 6 ? 0 : s == 6 ? 14 : s < 9 ? IV_K + 1 : s < 11 ? IV_K + s - 4 : s == 11 ? IV_SM : IV_V + s - 11; }
constexpr Tab make_tab() {
  Tab t{}; int n = 0;
  auto add = [&](int iv, int pos, int kind, int idx, int pc) { t.op[n] = Op{iv, pos, kind, idx, pc}; ++n; };
  for (int i = 0; i < 2; ++i) add(-1, 0, OP_X, 0, i);
  for (int i = 0; i < 2; ++i) add(-1, 0, OP_W, 0, i);
  add(-1, 0, OP_EXT, 0, 0);
  for (int i = 0; i < 2; ++i) add(-1, 1, OP_W, 1, i);
  for (int i = 0; i < 2; ++i) add(-1, 1, OP_X, 1, i);
  for (int i = 0; i < 4; ++i) add(0, 4 + i, OP_W, 2 + (i >> 1), i & 1);
  const int quota[33] = QUOTA;
  int sp = 0;
  for (int iv = 1; iv <= 32; ++iv) {
    int p0 = 2, p1 = 16;
    if (iv <= 10) {
      if ((iv & 1) == 0) { add(iv, 1, OP_X, iv / 2 + 1, 0); add(iv, 2, OP_X, iv / 2 + 1, 1); add(iv, 3, OP_W, iv + 3, 0); add(iv, 5, OP_W, iv + 3, 1); p0 = 6; }
      else { add(iv, 2, OP_W, iv + 3, 0); add(iv, 3, OP_W, iv + 3, 1); p0 = 5; }
      p1 = 8;
    } else if (iv <= 13) p1 = 8;
    else if (iv == 14 || iv == 32) p1 = 4;
    else if (iv == IV_SM) { p0 = 0; p1 = 1; }
    else if (iv == IV_V) p0 = 6;
    if (iv == PFIV) add(iv, 1, OP_PF, 0, 0);
    if (iv == IV_V + 5) add(iv, 1, OP_EP, 0, 0);
    const int q = quota[iv];
    for (int i = 0; i < q && sp < 76 && avail(sp >> 2) <= iv; ++i, ++sp) {
      const int s = sp >> 2;
      add(iv, p0 + (i * (p1 - p0)) / q, s < 8 ? OP_K : s < 16 ? OP_V : OP_O, s & 7, sp & 3);
    }
  }
  for (int j = 1; j <= 6; ++j) {
    if (j <= 2) for (int i = 0; i < 8; ++i) add(IV_O + j, i, OP_O, 1 + 2 * j + (i >> 2), i & 3);
    add(IV_O + j, 9, OP_ST, j - 1, 0); add(IV_O + j, 13, OP_ST, j - 1, 1);
  }
  t.n = n; return t;
}
constexpr Tab TAB = make_tab();
constexpr int op_first(int iv, int pos) { int i = 0; while (i < TAB.n && (TAB.op[i].iv < iv || (TAB.op[i].iv == iv && TAB.op[i].pos < pos))) ++i; return i; }
constexpr int inflight(int iv, int pos, int kind, int idx) {
  int last = -1, cnt = 0; for (int i = 0; i < TAB.n; ++i) if (TAB.op[i].kind == kind && TAB.op[i].idx == idx) { last = i; ++cnt; }
  const int lim = op_first(iv, pos);
  return (cnt != ((kind == OP_X || kind == OP_W || kind == OP_ST) ? 2 : kind >= OP_EXT ? 1 : 4) || last >= lim) ? -1 : lim - 1 - last; }
constexpr int cmin(int a, int b) { return a < b ? a : b; }

__global__ __launch_bounds__(512, 2)
void attn_fused(const char* __restrict__ xA, const char* __restrict__ kA, const char* __restrict__ vA,
                const char* __restrict__ wqA, const char* __restrict__ woA,
                const char* __restrict__ xB, const char* __restrict__ kB, const char* __restrict__ vB,
                const char* __restrict__ wqB, const char* __restrict__ woB, char* ws, char* out) {
  __shared__ __attribute__((aligned(1024))) char smem[LDS_BYTES];
  const unsigned lds0 = (unsigned)(uintptr_t)(LAS char*)smem;

  const int xcd = blockIdx.x & 7, idx = blockIdx.x >> 3;
  const int br = xcd >> 2, head = (xcd & 3) * 4 + (idx & 3), bat = idx >> 2, grp = br * NB + bat;
  const int tid = threadIdx.x, lane = tid & 63;
  const int wid = __builtin_amdgcn_readfirstlane(tid >> 6);
  const int r = lane & 31, h = lane >> 5, g = (lane >> 4) & 1, q4 = (lane >> 2) & 3, p = lane & 3;
  const int wl = wid & 3, wh = wid >> 2;
  unsigned stv = 0; (void)stv; STAMP(0); STAMPC(62);

  const char* xg  = (br ? xB : xA) + (size_t)bat * SQ * DM * 2;
  const char* kg  = (br ? kB : kA) + (size_t)(bat * NH + head) * SK * DH * 2;
  const char* vg  = (br ? vB : vA) + (size_t)(bat * NH + head) * SK * DH * 2;
  const char* wqg = (br ? wqB : wqA) + (size_t)head * DH * 2;
  const char* wog = (br ? woB : woA) + (size_t)head * DH * DM * 2;
  const __amdgpu_buffer_rsrc_t rsX  = __builtin_amdgcn_make_buffer_rsrc((void*)xg, 0, SQ * DM * 2, 0x00020000);
  const __amdgpu_buffer_rsrc_t rsK  = __builtin_amdgcn_make_buffer_rsrc((void*)kg, 0, SK * DH * 2, 0x00020000);
  const __amdgpu_buffer_rsrc_t rsV  = __builtin_amdgcn_make_buffer_rsrc((void*)vg, 0, SK * DH * 2, 0x00020000);
  const __amdgpu_buffer_rsrc_t rsWq = __builtin_amdgcn_make_buffer_rsrc((void*)wqg, 0, (DM - 1) * NH * DH * 2 + DH * 2, 0x00020000);
  const __amdgpu_buffer_rsrc_t rsWo = __builtin_amdgcn_make_buffer_rsrc((void*)wog, 0, DH * DM * 2, 0x00020000);
  const __amdgpu_buffer_rsrc_t rsP  = __builtin_amdgcn_make_buffer_rsrc((void*)(ws + WSOFF + CTRL_BYTES + (size_t)grp * GRP_BYTES), 0, NH * PART_BYTES, 0x00020000);

  const unsigned swzT = (unsigned)((r ^ ((2 * (wid & 1) + h) << 2)) << 4);
  const unsigned voWq = (unsigned)h * (NH * DH * 2) + swzT;
  const unsigned voV  = (unsigned)h * 512u + swzT;
  const unsigned voK  = (unsigned)h * 512u + (unsigned)((r ^ h ^ ((2 * wid) & 15)) << 4);
  const unsigned voX  = (unsigned)(lane >> 3) * (DM * 2) + (unsigned)(((lane & 7) ^ (4 * (wid & 1) + (lane >> 4))) << 4);
  unsigned voWo = 0;

  const int rot1 = ROT1, rot4 = ROT4;
  auto rotc = [](int j, int rot) { const int c = j + rot; return c >= 7 ? c - 7 : c; };
  const int rot6 = (bat + 2 * (head & 3)) % 6;
  auto rot1c = [&](int j, int) { const int c = j + rot6; return j == 0 ? 0 : c >= 7 ? c - 6 : c; };
  const unsigned ldw = lds0 + wid * 1024;
  const unsigned frag = lds0 + lane * 16;
  unsigned so = 0;
  u32x4 kland[NLAND * 4]; unsigned pfr = 0;
  f32x16 C[7];
  u32x4 ow[2];
  float inv = 0.f;
  unsigned pw = 0;
  u64 word = 0;

  auto issue = [&](auto IV, auto POS) {
    constexpr int f = op_first(IV.v, POS.v), c = op_first(IV.v, POS.v + 1) - f;
    sfor<c>([&](auto I) { constexpr Op o = TAB.op[f + I.v];
      if constexpr (o.kind == OP_X) {
        if constexpr (o.pc == 0) { so = (unsigned)(wid * 8 * DM * 2 + rot1c(o.idx, rot1) * 128); PINS(so); }
        dma(rsX, voX, so + o.pc * (64 * DM * 2), ldw + XBUF(o.idx) + o.pc * 8192);
      } else if constexpr (o.kind == OP_W) {
        if constexpr (o.pc == 0) { so = (unsigned)((rot1c(o.idx >> 1, rot1) * 64 + (o.idx & 1) * 32 + 2 * wid) * (NH * DH * 2)); PINS(so); }
        dma(rsWq, voWq, so + o.pc * 16 * (NH * DH * 2), ldw + WBUF(o.idx) + o.pc * 8192);
      } else if constexpr (o.kind == OP_K) {
        if constexpr (o.idx >= 2 && o.idx < 2 + NLAND) kland[(o.idx - 2) * 4 + o.pc] = ld_nt(rsK, voK, (unsigned)(wid * 1024 + o.idx * SLOT + o.pc * 8192));
        else dma_nt(rsK, voK, (unsigned)(wid * 1024 + o.idx * SLOT + o.pc * 8192), ldw + KSLOT(o.idx) + o.pc * 8192);
      } else if constexpr (o.kind == OP_V) {
        dma_nt(rsV, voV, (unsigned)(wid * 1024 + o.idx * SLOT + o.pc * 8192), ldw + VSLOT(o.idx) + o.pc * 8192);
      } else if constexpr (o.kind == OP_O) {
        if constexpr (o.pc == 0) { so = (unsigned)(wid * 8 * DM * 2 + rotc(o.idx, rot4) * 128); PINS(so); }
        dma_o(rsWo, voWo, so + o.pc * (64 * DM * 2), ldw + OSLOT(o.idx) + o.pc * 8192);
      } else if constexpr (o.kind == OP_PF) {
        pfr = ld_def(rsWo, lane < 28 ? (unsigned)lane * 128u : 0x40000000u, (unsigned)((bat * 8 + wid) * 28 * 128));
      } else if constexpr (o.kind == OP_EP) {
        word = ld_sc1(ws + grp * 256, 0u);
      } else if constexpr (o.kind == OP_ST) {
        __builtin_amdgcn_raw_buffer_store_b128(ow[o.pc], rsP, pw + o.idx * 16384 + o.pc * 1024, 0, STAUX  );
      } }); };
  auto park = [&](auto T, auto I) { wr128(frag + wid * 1024 + KSLOT(T.v) + I.v * 8192, kland[(T.v - 2) * 4 + I.v]); };

  issue(IC<-1>{}, IC<0>{});
  {
    u64 pcv; unsigned len;
    asm volatile("s_getpc_b64 %0\n\t.Lattn_code_begin:\n\ts_mov_b32 %1, .Lattn_code_end - .Lattn_code_begin" : "=s"(pcv), "=s"(len));
    const __amdgpu_buffer_rsrc_t rsC = __builtin_amdgcn_make_buffer_rsrc((void*)(pcv & ~127ull), 0, len & ~15u, 0x00020000);
    dma(rsC, frag - lds0, (unsigned)((wid * 32 + idx) * 1024), ldw + KSLOT(0));
  }
  lds_bar();
  issue(IC<-1>{}, IC<1>{});

  const unsigned trA = lds0 + (8 * h + q4) * 512 + (((8 * wl + 2 * g + (p & 1)) ^ (q4 << 2)) << 4) + (p >> 1) * 8;

  f32x16 acc[2][2];
  sfor<4>([&](auto i) { acc[i.v >> 1][i.v & 1] = f32x16{}; });
  unsigned tb = 0;
  auto putT = [&](int base, auto I) { constexpr int dt = I.v >> 1, qt = I.v & 1;
    if constexpr (I.v == 0) { tb = frag + base + (16 * wl + 2 * wh) * 1024; PIN(tb); }
    sfor<2>([&](auto s) { wr128(tb + ((2 * dt + s.v) * 4 + qt) * 1024, pack8(acc[dt][qt], s.v)); }); };
  {
    const unsigned xrd = lds0 + (64 * wh + r) * 128 + ((h ^ ((r >> 1) & 7)) << 4);
    bf16x8 a0[29], a1[29], b0[29], b1[29];
    auto rdA = [&](auto U) { constexpr int u = U.v, s = u >> 1, ks = u & 1, kk = u & 3;
      a0[u] = rdtr2(trA + WBUF(s) + ks * 8192, 2048); b0[u] = rd128((xrd ^ (kk << 5)) + XBUF(s >> 1)); };
    auto rdB = [&](auto U) { constexpr int u = U.v, s = u >> 1, ks = u & 1, kk = u & 3;
      b1[u] = rd128((xrd ^ (kk << 5)) + XBUF(s >> 1) + 4096); a1[u] = rdtr2((trA ^ 64) + WBUF(s) + ks * 8192, 2048); };
    wait_bar<cmin(inflight(0, 0, OP_X, 0), inflight(0, 0, OP_W, 0))>(); STAMP(1);
    rdA(IC<0>{}); rdB(IC<0>{}); SBAR();
    sfor<28>([&](auto U) { constexpr int u = U.v;
      constexpr int sn = (u + 1) >> 1, k0 = ((u + 1) & 1) * 4;
      if constexpr ((u & 1) == 1) {
        if constexpr (sn < 14) wait_bar<cmin(inflight(sn, 0, OP_X, sn >> 1), inflight(sn, 0, OP_W, sn < 14 ? sn : 0))>();
        else lds_bar();
        STAMP(1 + sn);
      }
      PRIO(3 - k0 / 2);
      acc[0][0] = MFMA(a0[u], b0[u], acc[0][0]); SBAR();
      if constexpr (u + 1 < 28) rdA(IC<u + 1>{});
      issue(IC<sn>{}, IC<k0>{}); SBAR();
      acc[0][1] = MFMA(a0[u], b1[u], acc[0][1]); SBAR();
      if constexpr (u + 1 < 28) rdB(IC<u + 1>{});
      issue(IC<sn>{}, IC<k0 + 1>{}); SBAR();
      PRIO(2 - k0 / 2);
      acc[1][0] = MFMA(a1[u], b0[u], acc[1][0]); SBAR(); issue(IC<sn>{}, IC<k0 + 2>{});
      if constexpr (u == 27) putT(QIMG, IC<0>{});
      SBAR();
      acc[1][1] = MFMA(a1[u], b1[u], acc[1][1]); SBAR(); issue(IC<sn>{}, IC<k0 + 3>{}); SBAR();
    });
  }
  putT(QIMG, IC<1>{}); putT(QIMG, IC<2>{}); putT(QIMG, IC<3>{});

  bf16x8 qf[16];
  f32x16 S[8];
  float mh[NE], lt[NE + 1], mx = -__builtin_inff(), ma = 0.f, nm = 0.f;
  auto tmax = [&](auto T, auto K) { constexpr int t = T.v;
    if constexpr (K.v == 0) { ma = max3(max3(S[t][0], S[t][1], S[t][2]), S[t][3], S[t][4]); ma = max3(ma, S[t][5], S[t][6]); mx = max3(mx, S[t][7], S[t][8]); PIN(ma); PIN(mx); }
    else { ma = max3(ma, S[t][9], S[t][10]); mx = max3(mx, S[t][11], S[t][12]); ma = max3(ma, S[t][13], S[t][14]); mx = max3(mx, S[t][15], ma); PIN(mx); } };
  auto estep = [&](auto N, auto LO, auto HI) { constexpr int n = N.v, lo = LO.v, hi = HI.v;
    if constexpr (n < hi) { constexpr int t = (n >> 4) & 7, e = n & 15;
      if constexpr (e == 0 && t < NE) { mh[t < NE ? t : 0] = mx * SCL; nm = -mh[t < NE ? t : 0]; }
      float x = fmaf(S[t][e], SCL, nm); PIN(x); S[t][e] = x; }
    if constexpr (n - 1 >= lo && n - 1 < hi) { constexpr int t = ((n - 1) >> 4) & 7, e = (n - 1) & 15; float x = ex2(S[t][e]); PIN(x); S[t][e] = x; }
    if constexpr (n - 2 >= lo && n - 2 < hi) { constexpr int t = ((n - 2) >> 4) & 7, e = (n - 2) & 15, a = t < NE ? t : NE;
      if constexpr (e == 0 && t <= NE) lt[a] = S[t][e]; else { lt[a] += S[t][e]; PIN(lt[a]); } } };
  {
    const int kr = (r & 19) | ((r & 4) << 1) | ((r & 8) >> 1);
    const unsigned krd = lds0 + (32 * wh + kr) * 512 + ((h ^ (kr & 15)) << 4);
    bf16x8 af[128];
    unsigned kb = 0;
    auto rd = [&](auto T, auto I) { constexpr int t = T.v, ks = KORD(I.v);
      if constexpr (t == 0) { if constexpr (I.v == 0) { tb = frag + QIMG + wl * 1024; PIN(tb); } qf[ks] = rd128(tb + ks * 4096); }
      af[16 * t + I.v] = rd128((kb ^ ((ks & 7) << 5)) + (ks >> 3) * 256); };
    auto mm = [&](auto T, auto I) { constexpr int t = T.v, i = I.v;
      if constexpr (i == 0) S[t] = MFMA(af[16 * t], qf[KORD(0)], f32x16{}); else S[t] = MFMA(af[16 * t + i], qf[KORD(i)], S[t]); };
    sfor<8>([&](auto T) { constexpr int t = T.v;
      wait_bar<cmin(63, cmin(inflight(IV_K + t, 0, OP_K, t), inflight(IV_K + t, 0, OP_K, (t >= 1 && t <= NLAND) ? t + 1 : t)))>(); STAMP(16 + t);
      kb = krd + KSLOT(t); PIN(kb);
      if constexpr (IV_K + t == PFIV + 2) { wait_vm<PFIV < 0 ? 0 : inflight(IV_K + t, 0, OP_PF, 0)>(); PIN(pfr); }
      sfor<16>([&](auto K) { constexpr int k = K.v;
        if constexpr ((k & 3) == 0) PRIO(3 - (k >> 2));
        if constexpr (k < 3) { if constexpr (t > 0) mm(IC<(t > 0 ? t - 1 : 0)>{}, IC<13 + k>{}); }
        else mm(T, IC<k - 3>{});
        SBAR();
        if constexpr (k < 2) { rd(T, IC<2 * k>{}); rd(T, IC<2 * k + 1>{}); }
        else if constexpr (k < 14) rd(T, IC<k + 2>{});
        issue(IC<IV_K + t>{}, K);
        if constexpr (t >= 1 && t <= NLAND && k >= 3 && k <= 12 && k % 3 == 0) park(IC<(t >= 1 && t <= NLAND) ? t + 1 : 2>{}, IC<(k / 3 - 1) & 3>{});
        if constexpr (t > 0 && (k == 5 || k == 6)) tmax(IC<(t > 0 ? t - 1 : 0)>{}, IC<(k - 5) & 1>{});
        if constexpr ((t > 1 || (t == 1 && k >= 7)) && k != 5 && k != 6) {
          constexpr int y = (t - 1) * 14 + (k < 5 ? k : k - 2) - 5, span = 7 * 14 - 5, tot = NE * 16 + 2, n0 = y * tot / span, n1 = (y + 1) * tot / span;
          sfor<n1 - n0>([&](auto i) { static_assert(((n0 + i.v) >> 4) < t || n0 + i.v >= NE * 16, "scores not ready");
            static_assert(((n0 + i.v) >> 4) != t - 1 || k >= 7 || n0 + i.v >= NE * 16, "max not taken yet");
            estep(IC<n0 + i.v>{}, IC<0>{}, IC<NE * 16>{}); }); }
        SBAR();
      });
    });
    mm(IC<7>{}, IC<13>{}); mm(IC<7>{}, IC<14>{}); mm(IC<7>{}, IC<15>{});
    STAMP(24);
  }
  tmax(IC<7>{}, IC<0>{}); tmax(IC<7>{}, IC<1>{});
  {
    const f32x2 m2 = xhalf(mx * SCL);
    const float mw = fmaxf(m2[0], m2[1]);
    if (h == 0) wr32(lds0 + PRING + ((2 * (1 - wh)) * 4 + wl) * 1024 + r * 4, mw);
    lds_bar(); STAMP(25);
    nm = -fmaxf(mw, rd32(lds0 + PRING + ((2 * wh) * 4 + wl) * 1024 + r * 4));
  }
  float fs = 1.f, ls = 0.f; u32x4 pv;
  unsigned pwr = frag + PRING + (8 * wh + wl) * 1024, prd = frag + PRING + 2 * wh * 1024; PIN(pwr); PIN(prd);
  auto putP = [&](auto T, auto Q) { constexpr int t = T.v, q = Q.v, e = 4 * q;
    if constexpr (q == 0 && t < NE) { fs = ex2(mh[t < NE ? t : 0] + nm); if constexpr (t == 0) ls = lt[0] * fs; else ls = fmaf(lt[t < NE ? t : 0], fs, ls); }
    if constexpr (t < NE) { pv[2 * (q & 1)] = pk(S[t][e] * fs, S[t][e + 1] * fs); pv[2 * (q & 1) + 1] = pk(S[t][e + 2] * fs, S[t][e + 3] * fs); }
    else { pv[2 * (q & 1)] = pk(S[t][e], S[t][e + 1]); pv[2 * (q & 1) + 1] = pk(S[t][e + 2], S[t][e + 3]); }
    if constexpr (q & 1) wr128(pwr + (t & 1) * 16384 + (q >> 1) * 4096, pv); else PIN(pv); };
  sfor<4>([&](auto q) { putP(IC<0>{}, q); });
  issue(IC<IV_SM>{}, IC<0>{});
  voWo = (unsigned)(lane >> 3) * (DM * 2) + (unsigned)(((lane & 7) ^ (g << 2)) << 4);

  sfor<4>([&](auto i) { acc[i.v >> 1][i.v & 1] = f32x16{}; });
  {
    bf16x8 a0[33], a1[33], b0[33], b1[33];
    unsigned vb = 0;
    auto rdA = [&](auto U) { constexpr int u = U.v, t = u >> 2, ks = u & 3;
      a0[u] = rdtr2(vb + ks * 8192, 2048); b0[u] = rd128(prd + (t & 1) * 16384 + ks * 4096); };
    auto rdB = [&](auto U) { constexpr int u = U.v, t = u >> 2, ks = u & 3;
      b1[u] = rd128(prd + (t & 1) * 16384 + ks * 4096 + 1024); a1[u] = rdtr2((vb ^ 64) + ks * 8192, 2048); };
    wait_bar<inflight(IV_V, 0, OP_V, 0)>(); STAMP(26);
    vb = trA + VSLOT(0); PIN(vb);
    rdA(IC<0>{}); rdB(IC<0>{}); SBAR();
    sfor<32>([&](auto U) { constexpr int u = U.v;
      constexpr int tn = (u + 1) >> 2, gi = (u + 1) & 3;
      if constexpr (gi == 0) {
        if constexpr (tn < 8) { wait_bar<inflight(IV_V + tn, 0, OP_V, tn < 8 ? tn : 0)>(); vb = trA + VSLOT(tn); PIN(vb); } else lds_bar();
        STAMP(26 + tn);
      }
      auto side = [&](auto Z) { constexpr int z = 2 * gi + Z.v;
        if constexpr (tn < 7 && z >= 2 && z < 6) putP(IC<(tn < 7 ? tn + 1 : 0)>{}, IC<(z - 2) & 3>{});
        if constexpr (tn == 7 && z == 0) { const f32x2 l2 = xhalf(ls + lt[NE]); ls = l2[0] + l2[1]; if (h == 0) wr32(lds0 + PRING + wid * 128 + r * 4, ls); }
        if constexpr (tn == 7 && z == 3) lds_bar();
        if constexpr (tn == 7 && z == 4) { inv = 1.0f / (ls + rd32(lds0 + PRING + (wid ^ 4) * 128 + r * 4)); PIN(inv); }
        if constexpr (u == 31 && Z.v == 0) putT(OIMG, IC<0>{}); };
      auto late = [&](auto Y) { constexpr int y = 4 * u + Y.v, tot = (8 - NE) * 16 + 2, e0 = y < LSPAN ? y * tot / LSPAN : tot, e1 = y < LSPAN ? (y + 1) * tot / LSPAN : tot;
        sfor<e1 - e0>([&](auto i) { constexpr int n = NE * 16 + e0 + i.v; static_assert(n < NE * 16 + 2 || y < 16 * (((n - 2) >> 4) - 1) + 2, "P tile needed earlier");
          estep(IC<n>{}, IC<NE * 16>{}, IC<128>{}); }); };
      PRIO(3 - gi);
      acc[0][0] = MFMA(a0[u], b0[u], acc[0][0]); SBAR();
      if constexpr (u + 1 < 32) rdA(IC<u + 1>{});
      late(IC<0>{});
      issue(IC<IV_V + tn>{}, IC<4 * gi>{}); SBAR();
      acc[0][1] = MFMA(a0[u], b1[u], acc[0][1]); SBAR();
      if constexpr (u + 1 < 32) rdB(IC<u + 1>{});
      late(IC<1>{});
      issue(IC<IV_V + tn>{}, IC<4 * gi + 1>{}); SBAR();
      acc[1][0] = MFMA(a1[u], b0[u], acc[1][0]); SBAR();
      issue(IC<IV_V + tn>{}, IC<4 * gi + 2>{}); late(IC<2>{}); side(IC<0>{}); SBAR();
      acc[1][1] = MFMA(a1[u], b1[u], acc[1][1]); SBAR();
      issue(IC<IV_V + tn>{}, IC<4 * gi + 3>{}); late(IC<3>{}); side(IC<1>{}); SBAR();
    });
  }
  putT(OIMG, IC<1>{}); putT(OIMG, IC<2>{}); putT(OIMG, IC<3>{});

  GAS u64* ctrl = (GAS u64*)(ws + grp * 256);
  u64 tag = 0;
  {
    const unsigned trW = lds0 + (8 * h + q4) * 128 + (((4 * wh + 2 * g + (p & 1)) ^ ((q4 >> 1) << 2)) << 4) + (p >> 1) * 8;
    pw = (unsigned)(head * PART_BYTES + wid * 2048) + (frag - lds0);
    bf16x8 af[112];
    unsigned wb = 0;
    auto rd = [&](auto J, auto I) { constexpr int j = J.v, ks = I.v;
      if constexpr (j == 0) { if constexpr (ks == 0) { tb = frag + OIMG + wl * 1024; PIN(tb); } qf[ks] = rd128(tb + ks * 4096); }
      af[16 * j + ks] = rdtr2(wb + ks * 2048, 512); };
    auto mm = [&](auto J, auto I) { constexpr int j = J.v, i = I.v;
      if constexpr (i == 0) C[j] = MFMA(af[16 * j], qf[0], f32x16{}); else C[j] = MFMA(af[16 * j + i], qf[i], C[j]); };
    sfor<7>([&](auto J) { constexpr int j = J.v;
      wait_bar<cmin(inflight(IV_O + j, 0, OP_O, j), j == 0 ? inflight(IV_O, 0, OP_EP, 0) : 63)>();
      STAMP(35 + j);
      if constexpr (j == 0) {
        PIN(word);
        const u64 e = ((u64)(unsigned)__builtin_amdgcn_readfirstlane((unsigned)(word >> 32)) << 32) | (unsigned)__builtin_amdgcn_readfirstlane((unsigned)word);
        tag = e * 0x9E3779B97F4A7C15ull + 0x632BE59BD9B4E019ull;
      }
      wb = trW + OSLOT(j); PIN(wb);
      sfor<16>([&](auto K) { constexpr int k = K.v;
        if constexpr ((k & 3) == 0) PRIO(3 - (k >> 2));
        if constexpr (k < 3) { if constexpr (j > 0) mm(IC<(j > 0 ? j - 1 : 0)>{}, IC<13 + k>{}); }
        else mm(J, IC<k - 3>{});
        SBAR();
        if constexpr (k < 2) { rd(J, IC<2 * k>{}); rd(J, IC<2 * k + 1>{}); }
        else if constexpr (k < 14) rd(J, IC<k + 2>{});
        issue(IC<IV_O + j>{}, K);
        if constexpr (j > 0 && k >= 5 && k < 13) { constexpr int e = k - 5, jp = j > 0 ? j - 1 : 0;
          unsigned w = pk(C[jp][2 * e] * inv, C[jp][2 * e + 1] * inv); PIN(w); ow[e >> 2][e & 3] = w; }
        SBAR();
      });
    });
    mm(IC<6>{}, IC<13>{}); mm(IC<6>{}, IC<14>{}); mm(IC<6>{}, IC<15>{});
    sfor<16>([&](auto e) { C[6][e.v] *= inv; });
    sfor<2>([&](auto s) { __builtin_amdgcn_raw_buffer_store_b128(pack8(C[6], s.v), rsP, pw + 6 * 16384 + s.v * 1024, 0, STAUX); });
  }
  STAMP(42);

  volatile LAS unsigned* ready = (volatile LAS unsigned*)(uintptr_t)(lds0 + OSLOT(2));
  if (wid == 7) {
    *ready = 1u << head;
    word = ld_sc1(ws + grp * 256 + 128, (frag - lds0) >> 1 & 0x78u);
    wait_bar<1>();
    if (lane == 0) __hip_atomic_store(ctrl + 16 + head, tag, RLX_AGENT);
    wait_vm<1>(); PIN(word);
    unsigned done = ((unsigned)__ballot(word == tag) & 0xffffu) | (1u << head);
    *ready = done;
#pragma unroll 1
    for (unsigned spins = 0; spins < (1u << 18) && done != 0xffffu; ++spins) {
      done = ((unsigned)__ballot(__hip_atomic_load(ctrl + 16 + (lane & 15), RLX_AGENT) == tag) & 0xffffu) | (1u << head);
      *ready = done;
      __builtin_amdgcn_s_sleep(1);
    }
    if (lane == 0 && head == 0 && done == 0xffffu) __hip_atomic_store(ctrl, tag, RLX_AGENT);
  } else {
    wait_bar<0>();
    STAMP(43);
    u32x4 v[NH];
    gather16(v, rsP, (unsigned)((wid * 16 + head) * 1024) + (frag - lds0), lds0 + OSLOT(2));
    STAMP(44);
    float s[8];
    sfor<8>([&](auto i) { s[i.v] = 0.f; });
    sfor<NH>([&](auto hh) { sfor<4>([&](auto i) {
      s[2 * i.v] += __uint_as_float(v[hh.v][i.v] << 16); s[2 * i.v + 1] += __uint_as_float(v[hh.v][i.v] & 0xffff0000u); }); });
    const int q = 32 * ((head >> 1) & 3) + r, n = 64 * rotc(wid, rot4) + 32 * (head >> 3) + 16 * (head & 1) + 8 * h;
    const __amdgpu_buffer_rsrc_t rsO = __builtin_amdgcn_make_buffer_rsrc((void*)(out + (size_t)bat * SQ * (2 * DM) * 2), 0, SQ * (2 * DM) * 2, 0x00020000);
    __builtin_amdgcn_raw_buffer_store_b128((u32x4){pk(s[0], s[1]), pk(s[2], s[3]), pk(s[4], s[5]), pk(s[6], s[7])}, rsO, (unsigned)((q * (2 * DM) + br * DM + n) * 2), 0, OUTAUX);
    STAMP(45);
  }
  asm volatile(".Lattn_code_end:");
}

extern "C" void kernel_launch(void* const* d_in, const int* in_sizes, int n_in,
                              void* d_out, int out_size, void* d_ws, size_t ws_size,
                              hipStream_t stream) {
  attn_fused<<<256, 512, 0, stream>>>((const char*)d_in[0], (const char*)d_in[1], (const char*)d_in[2], (const char*)d_in[3], (const char*)d_in[4],
                                      (const char*)d_in[5], (const char*)d_in[6], (const char*)d_in[7], (const char*)d_in[8], (const char*)d_in[9],
                                      (char*)d_ws, (char*)d_out);
}
```

```cpp
#include <hip/hip_runtime.h>
#include <hip/hip_bf16.h>
#include <stdint.h>

typedef short    bf16x8 __attribute__((ext_vector_type(8)));
typedef short    s16x4  __attribute__((ext_vector_type(4)));
typedef float    f32x4  __attribute__((ext_vector_type(4)));
typedef float    f32x2  __attribute__((ext_vector_type(2)));
typedef __bf16   bf16x2 __attribute__((ext_vector_type(2)));
typedef unsigned u32x4  __attribute__((ext_vector_type(4)));
typedef unsigned long long u64;
#define LAS __attribute__((address_space(3)))
#define GAS __attribute__((address_space(1)))

constexpr int NB = 8, SQ = 128, DM = 448, NH = 16, SK = 512, DH = 256;
constexpr int SLOT = 32768;
constexpr int LDS_BYTES = 5 * SLOT;
constexpr int PART_BYTES = SQ * DM * 2;
constexpr int CTRL_BYTES = 16 * 256;
constexpr float SCL = 0.0625f * 1.4426950408889634f;

#define STAMP(i) do {} while (0)
#define STAMPC(i) do {} while (0)
#define ROT4 ((bat & 1) * 3)

#define DI __device__ __forceinline__
#define MFMA(a, b, c) __builtin_amdgcn_mfma_f32_16x16x32_bf16((a), (b), (c), 0, 0, 0)
#define SBAR() __builtin_amdgcn_sched_barrier(0)
#define PIN(x) asm volatile("" : "+v"(x))
#define PINS(x) asm volatile("" : "+s"(x))
#define PRIO(n) __builtin_amdgcn_s_setprio(n)
#define RLX_AGENT __ATOMIC_RELAXED, __HIP_MEMORY_SCOPE_AGENT

template <int I> struct IC { static constexpr int v = I; };
template <int I, int N, class F> DI void sfor_(F& f) { if constexpr (I < N) { f(IC<I>{}); sfor_<I + 1, N>(f); } }
template <int N, class F> DI void sfor(F f) { sfor_<0, N>(f); }

template <int N> DI void wait_bar() { static_assert(N >= 0 && N < 64, "vmcnt"); SBAR(); asm volatile("s_waitcnt vmcnt(%0) lgkmcnt(0)\n\ts_barrier" :: "i"(N) : "memory"); SBAR(); }
template <int N> DI void wait_vm()  { static_assert(N >= 0 && N < 64, "vmcnt"); asm volatile("s_waitcnt vmcnt(%0)" :: "i"(N) : "memory"); }
DI void lds_bar() { SBAR(); asm volatile("s_waitcnt lgkmcnt(0)\n\ts_barrier" ::: "memory"); SBAR(); }

DI void dma(__amdgpu_buffer_rsrc_t rs, unsigned voff, unsigned soff, unsigned ldsb) {
  asm volatile("s_mov_b32 m0, %0\n\ts_nop 0\n\tbuffer_load_dwordx4 %1, %2, %3 offen lds"
               :: "s"(ldsb), "v"(voff), "s"(rs), "s"(soff) : "memory");
}
DI void dma_nt(__amdgpu_buffer_rsrc_t rs, unsigned voff, unsigned soff, unsigned ldsb) {
  asm volatile("s_mov_b32 m0, %0\n\ts_nop 0\n\tbuffer_load_dwordx4 %1, %2, %3 offen nt lds"
               :: "s"(ldsb), "v"(voff), "s"(rs), "s"(soff) : "memory");
}
DI u32x4 ld_nt(__amdgpu_buffer_rsrc_t rs, unsigned voff, unsigned soff) {
  u32x4 v; asm volatile("buffer_load_dwordx4 %0, %1, %2, %3 offen nt" : "=v"(v) : "v"(voff), "s"(rs), "s"(soff) : "memory"); return v;
}
DI u64 ld_sc1(const void* base, unsigned voff) {
  u64 v; asm volatile("global_load_dwordx2 %0, %1, %2 sc1" : "=v"(v) : "v"(voff), "s"(base) : "memory"); return v;
}
DI void gather16(u32x4 (&v)[16], __amdgpu_buffer_rsrc_t rs, unsigned off, unsigned ra) {
  static_assert(PART_BYTES == 114688, "offsets below");
  unsigned done, cnt, f, so, t;
  asm volatile(
      "s_mov_b32 %[done], 0\n\ts_mov_b32 %[cnt], 0x400000\n"
      ".Lgl_%=:\n\tds_read_b32 %[t], %[ra]\n\ts_waitcnt lgkmcnt(0)\n\tv_readfirstlane_b32 %[f], %[t]\n\t"
      "s_andn2_b32 %[f], %[f], %[done]\n\ts_cbranch_scc0 .Lgi_%=\n\t"
      "s_bitcmp1_b32 %[f], 0\n\ts_cbranch_scc0 .Lg0_%=\n\ts_mov_b32 %[so], 0\n\tbuffer_load_dwordx4 %0, %[off], %[rs], %[so] offen sc1\n.Lg0_%=:\n\t"
      "s_bitcmp1_b32 %[f], 1\n\ts_cbranch_scc0 .Lg1_%=\n\ts_mov_b32 %[so], 114688\n\tbuffer_load_dwordx4 %1, %[off], %[rs], %[so] offen sc1\n.Lg1_%=:\n\t"
      "s_bitcmp1_b32 %[f], 2\n\ts_cbranch_scc0 .Lg2_%=\n\ts_mov_b32 %[so], 229376\n\tbuffer_load_dwordx4 %2, %[off], %[rs], %[so] offen sc1\n.Lg2_%=:\n\t"
      "s_bitcmp1_b32 %[f], 3\n\ts_cbranch_scc0 .Lg3_%=\n\ts_mov_b32 %[so], 344064\n\tbuffer_load_dwordx4 %3, %[off], %[rs], %[so] offen sc1\n.Lg3_%=:\n\t"
      "s_bitcmp1_b32 %[f], 4\n\ts_cbranch_scc0 .Lg4_%=\n\ts_mov_b32 %[so], 458752\n\tbuffer_load_dwordx4 %4, %[off], %[rs], %[so] offen sc1\n.Lg4_%=:\n\t"
      "s_bitcmp1_b32 %[f], 5\n\ts_cbranch_scc0 .Lg5_%=\n\ts_mov_b32 %[so], 573440\n\tbuffer_load_dwordx4 %5, %[off], %[rs], %[so] offen sc1\n.Lg5_%=:\n\t"
      "s_bitcmp1_b32 %[f], 6\n\ts_cbranch_scc0 .Lg6_%=\n\ts_mov_b32 %[so], 688128\n\tbuffer_load_dwordx4 %6, %[off], %[rs], %[so] offen sc1\n.Lg6_%=:\n\t"
      "s_bitcmp1_b32 %[f], 7\n\ts_cbranch_scc0 .Lg7_%=\n\ts_mov_b32 %[so], 802816\n\tbuffer_load_dwordx4 %7, %[off], %[rs], %[so] offen sc1\n.Lg7_%=:\n\t"
      "s_bitcmp1_b32 %[f], 8\n\ts_cbranch_scc0 .Lg8_%=\n\ts_mov_b32 %[so], 917504\n\tbuffer_load_dwordx4 %8, %[off], %[rs], %[so] offen sc1\n.Lg8_%=:\n\t"
      "s_bitcmp1_b32 %[f], 9\n\ts_cbranch_scc0 .Lg9_%=\n\ts_mov_b32 %[so], 1032192\n\tbuffer_load_dwordx4 %9, %[off], %[rs], %[so] offen sc1\n.Lg9_%=:\n\t"
      "s_bitcmp1_b32 %[f], 10\n\ts_cbranch_scc0 .Lg10_%=\n\ts_mov_b32 %[so], 1146880\n\tbuffer_load_dwordx4 %10, %[off], %[rs], %[so] offen sc1\n.Lg10_%=:\n\t"
      "s_bitcmp1_b32 %[f], 11\n\ts_cbranch_scc0 .Lg11_%=\n\ts_mov_b32 %[so], 1261568\n\tbuffer_load_dwordx4 %11, %[off], %[rs], %[so] offen sc1\n.Lg11_%=:\n\t"
      "s_bitcmp1_b32 %[f], 12\n\ts_cbranch_scc0 .Lg12_%=\n\ts_mov_b32 %[so], 1376256\n\tbuffer_load_dwordx4 %12, %[off], %[rs], %[so] offen sc1\n.Lg12_%=:\n\t"
      "s_bitcmp1_b32 %[f], 13\n\ts_cbranch_scc0 .Lg13_%=\n\ts_mov_b32 %[so], 1490944\n\tbuffer_load_dwordx4 %13, %[off], %[rs], %[so] offen sc1\n.Lg13_%=:\n\t"
      "s_bitcmp1_b32 %[f], 14\n\ts_cbranch_scc0 .Lg14_%=\n\ts_mov_b32 %[so], 1605632\n\tbuffer_load_dwordx4 %14, %[off], %[rs], %[so] offen sc1\n.Lg14_%=:\n\t"
      "s_bitcmp1_b32 %[f], 15\n\ts_cbranch_scc0 .Lg15_%=\n\ts_mov_b32 %[so], 1720320\n\tbuffer_load_dwordx4 %15, %[off], %[rs], %[so] offen sc1\n.Lg15_%=:\n\t"
      "s_or_b32 %[done], %[done], %[f]\n\ts_cmpk_eq_u32 %[done], 0xffff\n\ts_cbranch_scc1 .Lge_%=\n"
      ".Lgi_%=:\n\ts_sleep 1\n\ts_sub_u32 %[cnt], %[cnt], 1\n\ts_cbranch_scc0 .Lgl_%=\n"
      ".Lge_%=:\n\ts_waitcnt vmcnt(0)"
      : "=&v"(v[0]), "=&v"(v[1]), "=&v"(v[2]), "=&v"(v[3]), "=&v"(v[4]), "=&v"(v[5]), "=&v"(v[6]), "=&v"(v[7]), "=&v"(v[8]), "=&v"(v[9]), "=&v"(v[10]), "=&v"(v[11]), "=&v"(v[12]), "=&v"(v[13]), "=&v"(v[14]), "=&v"(v[15]),
        [done] "=&s"(done), [cnt] "=&s"(cnt), [f] "=&s"(f), [so] "=&s"(so), [t] "=&v"(t)
      : [ra] "v"(ra), [off] "v"(off), [rs] "s"(rs) : "memory", "scc");
}
DI unsigned pk(float a, float b) {
  const bf16x2 v = __builtin_convertvector((f32x2){a, b}, bf16x2);
  return __builtin_bit_cast(unsigned, v);
}
DI u32x4 pack2(const f32x4& a, const f32x4& b) { return (u32x4){pk(a[0], a[1]), pk(a[2], a[3]), pk(b[0], b[1]), pk(b[2], b[3])}; }
DI u32x4 rowpair(u32x4 x) {
  auto a = __builtin_amdgcn_permlane16_swap(x[0], x[2], false, false);
  auto b = __builtin_amdgcn_permlane16_swap(x[1], x[3], false, false);
  return (u32x4){a[0], b[0], a[1], b[1]};
}
DI bf16x8 rd128(unsigned a) { return *(const LAS bf16x8*)(uintptr_t)a; }
DI void wr128(unsigned a, u32x4 v) { *(LAS u32x4*)(uintptr_t)a = v; }
DI f32x2 rd64(unsigned a) { return *(const LAS f32x2*)(uintptr_t)a; }
DI void wr64(unsigned a, f32x2 v) { *(LAS f32x2*)(uintptr_t)a = v; }
DI s16x4 rdtr(unsigned a) { return __builtin_amdgcn_ds_read_tr16_b64_v4i16((LAS s16x4*)(uintptr_t)a); }
DI bf16x8 rdtr2(unsigned a, int step) {
  const s16x4 lo = rdtr(a), hi = rdtr(a + step);
  return __builtin_shufflevector(lo, hi, 0, 1, 2, 3, 4, 5, 6, 7);
}
DI float ex2(float v) { return __builtin_amdgcn_exp2f(v); }
DI float max3(float a, float b, float c) { return fmaxf(fmaxf(a, b), c); }
template <bool MAX> DI float xrows(float v) {
  auto a = __builtin_amdgcn_permlane16_swap(__float_as_uint(v), __float_as_uint(v), false, false);
  v = MAX ? fmaxf(__uint_as_float(a[0]), __uint_as_float(a[1])) : __uint_as_float(a[0]) + __uint_as_float(a[1]);
  auto b = __builtin_amdgcn_permlane32_swap(__float_as_uint(v), __float_as_uint(v), false, false);
  return MAX ? fmaxf(__uint_as_float(b[0]), __uint_as_float(b[1])) : __uint_as_float(b[0]) + __uint_as_float(b[1]);
}

constexpr int XBUF(int j) { return (j & 1) * 49152; }
constexpr int WBUF(int s) { return ((s >> 1) & 1) * 49152 + 16384 + (s & 1) * 16384; }
constexpr int KSLOT(int t) { const int m[8] = {4, 3, 4, 3, 4, 3, 0, 1}; return m[t] * SLOT; }
constexpr int VSLOT(int t) { const int m[4] = {2, 4, 3, 1}; return m[t & 3] * SLOT; }
constexpr int OSLOT(int j) { const int m[7] = {2, 4, 3, 0, 1, 2, 4}; return m[j] * SLOT; }
constexpr int QIMG = SLOT, PRING = 0, OIMG = 0;
constexpr int EORD(int n) { return (n >> 2) + 4 * ((n >> 1) & 1); }

enum { OP_X, OP_W, OP_K, OP_V, OP_O, OP_ST, OP_EXT  , OP_EP };
enum { IV_K = 15, IV_SM = 23, IV_V = 24, IV_O = 33 };
struct Op { int iv, pos, kind, idx, pc; };
struct Tab { Op op[192]; int n; };
#define QUOTA { 0, 2, 0, 2, 0, 2, 0, 2, 0, 2, 0, 3, 2, 2, 2,  3, 3, 3, 3, 3, 3, 3, 3, 2, 3, 3, 3, 3, 3, 4, 4, 4, 1, 3}
constexpr int NE = 4, LSPAN = 96;
constexpr int NLAND = 4;
constexpr int avail(int s) {
  return s < 6 ? 0 : s == 6 ? 14 : s < 9 ? IV_K + 1 : s < 11 ? IV_K + s - 4 : s == 11 ? IV_SM : IV_V + s - 11; }
constexpr Tab make_tab() {
  Tab t{}; int n = 0;
  auto add = [&](int iv, int pos, int kind, int idx, int pc) { t.op[n] = Op{iv, pos, kind, idx, pc}; ++n; };
  for (int i = 0; i < 2; ++i) add(-1, 0, OP_X, 0, i);
  for (int i = 0; i < 2; ++i) add(-1, 0, OP_W, 0, i);
  add(-1, 0, OP_EXT, 0, 0);
  for (int i = 0; i < 2; ++i) add(-1, 1, OP_W, 1, i);
  for (int i = 0; i < 2; ++i) add(-1, 1, OP_X, 1, i);
  for (int i = 0; i < 4; ++i) add(0, 4 + i, OP_W, 2 + (i >> 1), i & 1);
  const int quota[34] = QUOTA;
  int sp = 0;
  for (int iv = 1; iv <= 33; ++iv) {
    int p0 = 2, p1 = 16;
    if (iv <= 10) {
      if ((iv & 1) == 0) { add(iv, 1, OP_X, iv / 2 + 1, 0); add(iv, 2, OP_X, iv / 2 + 1, 1); add(iv, 3, OP_W, iv + 3, 0); add(iv, 5, OP_W, iv + 3, 1); p0 = 6; }
      else { add(iv, 2, OP_W, iv + 3, 0); add(iv, 3, OP_W, iv + 3, 1); p0 = 5; }
      p1 = 8;
    } else if (iv <= 13) p1 = 8;
    else if (iv == 14 || iv == 32) p1 = 4;
    else if (iv == IV_SM) { p0 = 0; p1 = 1; }
    else if (iv == IV_V) p0 = 6;
    if (iv == IV_V + 5) add(iv, 1, OP_EP, 0, 0);
    const int q = quota[iv];
    for (int i = 0; i < q && sp < 76 && avail(sp >> 2) <= iv; ++i, ++sp) {
      const int s = sp >> 2;
      add(iv, p0 + (i * (p1 - p0)) / q, s < 8 ? OP_K : s < 16 ? OP_V : OP_O, s & 7, sp & 3);
    }
  }
  for (int j = 1; j <= 6; ++j) {
    if (j <= 2) for (int i = 0; i < 8; ++i) add(IV_O + j, i, OP_O, 1 + 2 * j + (i >> 2), i & 3);
    add(IV_O + j, 9, OP_ST, j - 1, 0); add(IV_O + j, 13, OP_ST, j - 1, 1);
  }
  t.n = n; return t;
}
constexpr Tab TAB = make_tab();
constexpr int op_first(int iv, int pos) { int i = 0; while (i < TAB.n && (TAB.op[i].iv < iv || (TAB.op[i].iv == iv && TAB.op[i].pos < pos))) ++i; return i; }
constexpr int inflight(int iv, int pos, int kind, int idx) {
  int last = -1, cnt = 0; for (int i = 0; i < TAB.n; ++i) if (TAB.op[i].kind == kind && TAB.op[i].idx == idx) { last = i; ++cnt; }
  const int lim = op_first(iv, pos);
  return (cnt != ((kind == OP_X || kind == OP_W || kind == OP_ST) ? 2 : kind >= OP_EXT ? 1 : 4) || last >= lim) ? -1 : lim - 1 - last; }
constexpr int cmin(int a, int b) { return a < b ? a : b; }

__global__ __launch_bounds__(512, 2)
void attn_fused(const char* __restrict__ xA, const char* __restrict__ kA, const char* __restrict__ vA,
                const char* __restrict__ wqA, const char* __restrict__ woA,
                const char* __restrict__ xB, const char* __restrict__ kB, const char* __restrict__ vB,
                const char* __restrict__ wqB, const char* __restrict__ woB, char* ws, char* out) {
  __shared__ __attribute__((aligned(1024))) char smem[LDS_BYTES];
  const unsigned lds0 = (unsigned)(uintptr_t)(LAS char*)smem;

  const int xcd = blockIdx.x & 7, idx = blockIdx.x >> 3;
  const int br = xcd >> 2, head = (xcd & 3) * 4 + (idx & 3), bat = idx >> 2, grp = br * NB + bat;
  const int tid = threadIdx.x, lane = tid & 63;
  const int wid = __builtin_amdgcn_readfirstlane(tid >> 6);
  const int r = lane & 31, h = lane >> 5, li = lane & 15, g4 = lane >> 4, q4 = (lane >> 2) & 3, p = lane & 3;
  const int wl = wid & 3, wh = wid >> 2;
  unsigned stv = 0; (void)stv; STAMP(0); STAMPC(62);

  const char* xg  = (br ? xB : xA) + (size_t)bat * SQ * DM * 2;
  const char* kg  = (br ? kB : kA) + (size_t)(bat * NH + head) * SK * DH * 2;
  const char* vg  = (br ? vB : vA) + (size_t)(bat * NH + head) * SK * DH * 2;
  const char* wqg = (br ? wqB : wqA) + (size_t)head * DH * 2;
  const char* wog = (br ? woB : woA) + (size_t)head * DH * DM * 2;
  const __amdgpu_buffer_rsrc_t rsX  = __builtin_amdgcn_make_buffer_rsrc((void*)xg, 0, SQ * DM * 2, 0x00020000);
  const __amdgpu_buffer_rsrc_t rsK  = __builtin_amdgcn_make_buffer_rsrc((void*)kg, 0, SK * DH * 2, 0x00020000);
  const __amdgpu_buffer_rsrc_t rsV  = __builtin_amdgcn_make_buffer_rsrc((void*)vg, 0, SK * DH * 2, 0x00020000);
  const __amdgpu_buffer_rsrc_t rsWq = __builtin_amdgcn_make_buffer_rsrc((void*)wqg, 0, (DM - 1) * NH * DH * 2 + DH * 2, 0x00020000);
  const __amdgpu_buffer_rsrc_t rsWo = __builtin_amdgcn_make_buffer_rsrc((void*)wog, 0, DH * DM * 2, 0x00020000);
  const __amdgpu_buffer_rsrc_t rsP  = __builtin_amdgcn_make_buffer_rsrc((void*)(ws + CTRL_BYTES + (size_t)grp * NH * PART_BYTES), 0, NH * PART_BYTES, 0x00020000);

  const unsigned voWq = (unsigned)h * (NH * DH * 2) + (unsigned)((r ^ ((2 * (wid & 1) + h + 4 * wh) << 1)) << 4);
  const unsigned voV  = (unsigned)h * 512u + (unsigned)((r ^ ((2 * wl + h) << 1)) << 4);
  const unsigned voK  = (unsigned)h * 512u + (unsigned)((r ^ h ^ ((2 * wid) & 15)) << 4);
  const unsigned voX  = (unsigned)(lane >> 3) * (DM * 2) + (unsigned)(((lane & 7) ^ (4 * (wid & 1) + g4)) << 4);
  unsigned voWo = 0;

  const int rot6 = (bat + 2 * (head & 3)) % 6, rot4 = ROT4;
  auto chunk1 = [&](int j) { const int c = j + rot6; return j == 0 ? 0 : c >= 7 ? c - 6 : c; };
  auto chunk4 = [&](int j) { const int c = j + rot4; return c >= 7 ? c - 7 : c; };
  const unsigned ldw = lds0 + wid * 1024;
  const unsigned frag = lds0 + lane * 16;
  unsigned so = 0;
  u32x4 kland[NLAND * 4];
  u32x4 ow[2];
  unsigned pw = 0;
  u64 word = 0;

  auto issue = [&](auto IV, auto POS) {
    constexpr int f = op_first(IV.v, POS.v), c = op_first(IV.v, POS.v + 1) - f;
    sfor<c>([&](auto I) { constexpr Op o = TAB.op[f + I.v];
      if constexpr (o.kind == OP_X) {
        if constexpr (o.pc == 0) { so = (unsigned)(wid * 8 * DM * 2 + chunk1(o.idx) * 128); PINS(so); }
        dma(rsX, voX, so + o.pc * (64 * DM * 2), ldw + XBUF(o.idx) + o.pc * 8192);
      } else if constexpr (o.kind == OP_W) {
        if constexpr (o.pc == 0) { so = (unsigned)((chunk1(o.idx >> 1) * 64 + (o.idx & 1) * 32 + 2 * wid) * (NH * DH * 2)); PINS(so); }
        dma(rsWq, voWq, so + o.pc * 16 * (NH * DH * 2), ldw + WBUF(o.idx) + o.pc * 8192);
      } else if constexpr (o.kind == OP_K) {
        if constexpr (o.idx >= 2 && o.idx < 2 + NLAND) kland[(o.idx - 2) * 4 + o.pc] = ld_nt(rsK, voK, (unsigned)(wid * 1024 + o.idx * SLOT + o.pc * 8192));
        else dma_nt(rsK, voK, (unsigned)(wid * 1024 + o.idx * SLOT + o.pc * 8192), ldw + KSLOT(o.idx) + o.pc * 8192);
      } else if constexpr (o.kind == OP_V) {
        dma_nt(rsV, voV, (unsigned)(wid * 1024 + o.idx * SLOT + o.pc * 8192), ldw + VSLOT(o.idx) + o.pc * 8192);
      } else if constexpr (o.kind == OP_O) {
        if constexpr (o.pc == 0) { so = (unsigned)(wid * 8 * DM * 2 + chunk4(o.idx) * 128); PINS(so); }
        dma(rsWo, voWo, so + o.pc * (64 * DM * 2), ldw + OSLOT(o.idx) + o.pc * 8192);
      } else if constexpr (o.kind == OP_EP) {
        word = ld_sc1(ws + grp * 256, 0u);
      } else if constexpr (o.kind == OP_ST) {
        __builtin_amdgcn_raw_buffer_store_b128(ow[o.pc], rsP, pw + o.idx * 16384 + o.pc * 1024, 0, 16  );
      } }); };
  auto park = [&](auto T, auto I) { wr128(frag + wid * 1024 + KSLOT(T.v) + I.v * 8192, kland[(T.v - 2) * 4 + I.v]); };

  issue(IC<-1>{}, IC<0>{});
  {
    u64 pcv; unsigned len;
    asm volatile("s_getpc_b64 %0\n\t.Lattn_code_begin:\n\ts_mov_b32 %1, .Lattn_code_end - .Lattn_code_begin" : "=s"(pcv), "=s"(len));
    const __amdgpu_buffer_rsrc_t rsC = __builtin_amdgcn_make_buffer_rsrc((void*)(pcv & ~127ull), 0, len & ~15u, 0x00020000);
    dma(rsC, frag - lds0, (unsigned)((wid * 32 + idx) * 1024), ldw + KSLOT(0));
  }
  lds_bar();
  issue(IC<-1>{}, IC<1>{});

  const unsigned trC = (unsigned)((((8 * wl + (p >> 1)) ^ ((q4 + 4 * (g4 & 1)) << 1)) << 4) + (p & 1) * 8);

  f32x4 acc[4][4];
  bf16x8 fa[17][4], fb[17][4];
  auto mm44 = [&](auto ST, auto Z) { constexpr int st = ST.v, z = Z.v, dt = (z & 1) + 2 * (z >> 2), qt = (z >= 2 && z < 6) ? 2 : 0;
    if constexpr (st == 0) { acc[dt][qt] = MFMA(fa[st][dt], fb[st][qt], f32x4{}); acc[dt][qt + 1] = MFMA(fa[st][dt], fb[st][qt + 1], f32x4{}); }
    else { acc[dt][qt] = MFMA(fa[st][dt], fb[st][qt], acc[dt][qt]); acc[dt][qt + 1] = MFMA(fa[st][dt], fb[st][qt + 1], acc[dt][qt + 1]); } };
  auto rd44 = [&](auto ST, auto Z, auto LAST, auto& rdA, auto& rdB) { constexpr int st = ST.v, z = Z.v;
    if constexpr (z < 2) rdA(ST, IC<2 + z>{});
    else if constexpr (z >= 4 && st < LAST.v) {
      if constexpr (z == 4) { rdA(IC<st + 1>{}, IC<0>{}); rdB(IC<st + 1>{}, IC<0>{}); }
      if constexpr (z == 5) { rdB(IC<st + 1>{}, IC<1>{}); rdA(IC<st + 1>{}, IC<1>{}); }
      if constexpr (z >= 6) rdB(IC<st + 1>{}, IC<z - 4>{});
    } };
  unsigned tb = 0;
  auto putT = [&](int base, auto SWAP, auto I) { constexpr int e2 = I.v >> 2, qt = I.v & 3;
    if constexpr (I.v == 0) { tb = frag + base + (16 * wl + 4 * wh) * 1024; PIN(tb); }
    const u32x4 x = pack2(acc[2 * e2][qt], acc[2 * e2 + 1][qt]);
    wr128(tb + (8 * e2 + qt) * 1024, SWAP.v ? rowpair(x) : x); };

  {
    const unsigned wrd = lds0 + (8 * g4 + q4) * 512 + trC;
    const unsigned xrd = lds0 + (64 * wh + li) * 128 + ((g4 ^ (li >> 1)) << 4);
    auto rdA = [&](auto S, auto D) { fa[S.v][D.v] = rdtr2((wrd ^ (D.v << 5)) + WBUF(S.v), 2048); };
    auto rdB = [&](auto S, auto Q) { fb[S.v][Q.v] = rd128((xrd ^ ((S.v & 1) << 6)) + XBUF(S.v >> 1) + Q.v * 2048); };
    wait_bar<cmin(inflight(0, 0, OP_X, 0), inflight(0, 0, OP_W, 0))>(); STAMP(1);
    sfor<4>([&](auto z) { rd44(IC<-1>{}, IC<4 + z.v>{}, IC<13>{}, rdA, rdB); }); SBAR();
    sfor<28>([&](auto U) { constexpr int u = U.v;
      constexpr int sn = (u + 1) >> 1, k0 = ((u + 1) & 1) * 4;
      if constexpr ((u & 1) == 1) {
        if constexpr (sn < 14) wait_bar<cmin(inflight(sn, 0, OP_X, sn >> 1), inflight(sn, 0, OP_W, sn < 14 ? sn : 0))>();
        else lds_bar();
        STAMP(1 + sn);
      }
      sfor<4>([&](auto Y) { constexpr int y = Y.v;
        if constexpr ((y & 1) == 0) PRIO(3 - (k0 + y) / 2);
        mm44(IC<(u >> 1)>{}, IC<(u & 1) * 4 + y>{}); SBAR();
        rd44(IC<(u >> 1)>{}, IC<(u & 1) * 4 + y>{}, IC<13>{}, rdA, rdB);
        issue(IC<sn>{}, IC<k0 + y>{});
        if constexpr (u == 27) putT(QIMG, IC<1>{}, Y);
        SBAR(); });
    });
  }
  putT(QIMG, IC<1>{}, IC<6>{}); putT(QIMG, IC<1>{}, IC<7>{}); putT(QIMG, IC<1>{}, IC<4>{}); putT(QIMG, IC<1>{}, IC<5>{});

  bf16x8 qf[8][2];
  f32x4 S[8][2][2];
  float mx[2] = {-__builtin_inff(), -__builtin_inff()}, mh[2], nm[2], lt[2][2];
  auto tmax = [&](auto T, auto Q) { constexpr int t = T.v, q = Q.v;
    const float a = max3(S[t][0][q][0], S[t][0][q][1], S[t][0][q][2]), b = max3(S[t][1][q][0], S[t][1][q][1], S[t][1][q][2]);
    mx[q] = max3(max3(mx[q], S[t][0][q][3], S[t][1][q][3]), a, b); PIN(mx[q]); };
  auto estep = [&](auto N, auto LO, auto HI) { constexpr int n = N.v, lo = LO.v, hi = HI.v;
    if constexpr (n < hi) { constexpr int t = (n >> 4) & 7, q = (n >> 3) & 1, kt = (n >> 2) & 1, e = n & 3;
      float x = fmaf(S[t][kt][q][e], SCL, nm[q]); PIN(x); S[t][kt][q][e] = x; }
    if constexpr (n - 1 >= lo && n - 1 < hi) { constexpr int m = n - 1, t = (m >> 4) & 7, q = (m >> 3) & 1, kt = (m >> 2) & 1, e = m & 3;
      float x = ex2(S[t][kt][q][e]); PIN(x); S[t][kt][q][e] = x; }
    if constexpr (n - 2 >= lo && n - 2 < hi) { constexpr int m = n - 2, t = (m >> 4) & 7, q = (m >> 3) & 1, kt = (m >> 2) & 1, e = m & 3, a = lo > 0;
      if constexpr (m - lo < 16 && (m & 7) == 0) lt[a][q] = S[t][kt][q][e]; else { lt[a][q] += S[t][kt][q][e]; PIN(lt[a][q]); } } };
  {
    const unsigned krd = lds0 + (32 * wh + li) * 512 + (((2 * (g4 & 1) + (g4 >> 1)) ^ li) << 4);
    bf16x8 af[128];
    unsigned kb = 0;
    auto rd = [&](auto T, auto I) { constexpr int t = T.v, e = EORD(I.v), kt = I.v & 1;
      if constexpr (t == 0 && kt == 0) { if constexpr (I.v == 0) { tb = frag + QIMG + 2 * wl * 1024; PIN(tb); }
        qf[e][0] = rd128(tb + e * 8192); qf[e][1] = rd128(tb + e * 8192 + 1024); }
      af[16 * t + I.v] = rd128((kb ^ ((e & 3) << 6)) + kt * 8192 + (e >> 2) * 256); };
    auto mm = [&](auto T, auto I) { constexpr int t = T.v, n = I.v, e = EORD(n), kt = n & 1;
      sfor<2>([&](auto q) { if constexpr (n < 2) S[t][kt][q.v] = MFMA(af[16 * t + n], qf[e][q.v], f32x4{}); else S[t][kt][q.v] = MFMA(af[16 * t + n], qf[e][q.v], S[t][kt][q.v]); }); };
    sfor<8>([&](auto T) { constexpr int t = T.v;
      wait_bar<cmin(63, cmin(inflight(IV_K + t, 0, OP_K, t), inflight(IV_K + t, 0, OP_K, (t >= 1 && t <= NLAND) ? t + 1 : t)))>(); STAMP(16 + t);
      kb = krd + KSLOT(t); PIN(kb);
      sfor<16>([&](auto K) { constexpr int k = K.v;
        if constexpr ((k & 3) == 0) PRIO(3 - (k >> 2));
        if constexpr (k < 3) { if constexpr (t > 0) mm(IC<(t > 0 ? t - 1 : 0)>{}, IC<13 + k>{}); }
        else mm(T, IC<k - 3>{});
        SBAR();
        if constexpr (k < 2) { rd(T, IC<2 * k>{}); rd(T, IC<2 * k + 1>{}); }
        else if constexpr (k < 14) rd(T, IC<k + 2>{});
        issue(IC<IV_K + t>{}, K);
        if constexpr (t >= 1 && t <= NLAND && k >= 3 && k <= 12 && k % 3 == 0) park(IC<(t >= 1 && t <= NLAND) ? t + 1 : 2>{}, IC<(k / 3 - 1) & 3>{});
        if constexpr (t > 0 && (k == 4 || k == 5)) tmax(IC<(t > 0 ? t - 1 : 0)>{}, IC<k & 1>{});
        if constexpr (t == NE && k == 6) { sfor<2>([&](auto q) { mh[q.v] = mx[q.v] * SCL; nm[q.v] = -mh[q.v]; }); }
        if constexpr ((t > NE || (t == NE && k >= 6)) && k != 4 && k != 5) {
          constexpr int y = (t - NE) * 14 + (k < 4 ? k : k - 2) - 4, span = (8 - NE) * 14 - 4, tot = NE * 16 + 2, n0 = y * tot / span, n1 = (y + 1) * tot / span;
          sfor<n1 - n0>([&](auto i) { estep(IC<n0 + i.v>{}, IC<0>{}, IC<NE * 16>{}); }); }
        SBAR();
      });
    });
    mm(IC<7>{}, IC<13>{}); mm(IC<7>{}, IC<14>{}); mm(IC<7>{}, IC<15>{});
    STAMP(24);
  }
  tmax(IC<7>{}, IC<0>{}); tmax(IC<7>{}, IC<1>{});
  float fs[2];
  {
    const f32x2 mw = {xrows<true>(mx[0] * SCL), xrows<true>(mx[1] * SCL)};
    if (g4 == 0) wr64(lds0 + PRING + ((1 - wh) * 8 + 2 * wl) * 1024 + li * 8, mw);
    lds_bar(); STAMP(25);
    const f32x2 o = rd64(lds0 + PRING + (wh * 8 + 2 * wl) * 1024 + li * 8);
    sfor<2>([&](auto q) { nm[q.v] = -fmaxf(mw[q.v], o[q.v]); fs[q.v] = ex2(mh[q.v] + nm[q.v]); });
  }
  u32x4 pv;
  unsigned pwr = frag + PRING + (8 * wh + 2 * wl) * 1024, prd = frag + PRING + 4 * wh * 1024; PIN(pwr); PIN(prd);
  auto putP = [&](auto T, auto Cq) { constexpr int t = T.v, q = Cq.v >> 1, kt = Cq.v & 1;
    f32x4 x = S[t][kt][q];
    if constexpr (t < NE) x *= fs[q];
    pv[2 * kt] = pk(x[0], x[1]); pv[2 * kt + 1] = pk(x[2], x[3]);
    if constexpr (kt) wr128(pwr + (t & 1) * 16384 + q * 1024, pv); else PIN(pv); };
  sfor<4>([&](auto c) { putP(IC<0>{}, c); });
  issue(IC<IV_SM>{}, IC<0>{});
  voWo = (unsigned)(lane >> 3) * (DM * 2) + (unsigned)(((lane & 7) ^ (g4 << 1)) << 4);

  float inv[2];
  {
    const unsigned vrd = lds0 + (4 * g4 + q4) * 512 + trC;
    unsigned vb = 0;
    auto rdA = [&](auto S_, auto D) { fa[S_.v][D.v] = rdtr2((vb ^ (D.v << 5)) + (S_.v & 1) * 16384, 8192); };
    auto rdB = [&](auto S_, auto Q) { fb[S_.v][Q.v] = rd128(prd + ((S_.v >> 1) & 1) * 16384 + (S_.v & 1) * 8192 + Q.v * 1024); };
    wait_bar<inflight(IV_V, 0, OP_V, 0)>(); STAMP(26);
    vb = vrd + VSLOT(0); PIN(vb);
    sfor<4>([&](auto z) { rd44(IC<-1>{}, IC<4 + z.v>{}, IC<15>{}, rdA, rdB); }); SBAR();
    sfor<32>([&](auto U) { constexpr int u = U.v;
      constexpr int tn = (u + 1) >> 2, gi = (u + 1) & 3;
      if constexpr (gi == 0) {
        if constexpr (tn < 8) { wait_bar<inflight(IV_V + tn, 0, OP_V, tn < 8 ? tn : 0)>(); vb = vrd + VSLOT(tn); PIN(vb); } else lds_bar();
        STAMP(26 + tn);
      }
      PRIO(3 - gi);
      sfor<4>([&](auto Y) { constexpr int y = Y.v, pos = 4 * gi + y, ys = 4 * u + y;
        mm44(IC<(u >> 1)>{}, IC<(u & 1) * 4 + y>{}); SBAR();
        rd44(IC<(u >> 1)>{}, IC<(u & 1) * 4 + y>{}, IC<15>{}, rdA, rdB);
        issue(IC<IV_V + tn>{}, IC<pos>{});
        { constexpr int tot = (8 - NE) * 16 + 2, e0 = ys < LSPAN ? ys * tot / LSPAN : tot, e1 = ys < LSPAN ? (ys + 1) * tot / LSPAN : tot;
          sfor<e1 - e0>([&](auto i) { constexpr int n = NE * 16 + e0 + i.v; static_assert(n < NE * 16 + 2 || ys < 16 * (((n - 2) >> 4) - 1) + 2, "P tile needed earlier");
            estep(IC<n>{}, IC<NE * 16>{}, IC<128>{}); }); }
        if constexpr (tn < 7 && (pos == 6 || pos == 7 || pos == 10 || pos == 11)) putP(IC<(tn < 7 ? tn + 1 : 0)>{}, IC<(pos & 1) + ((pos >> 3) << 1)>{});
        if constexpr (tn == 7 && pos == 2) { sfor<2>([&](auto q) { inv[q.v] = xrows<false>(fmaf(lt[0][q.v], fs[q.v], lt[1][q.v])); });
          if (g4 == 0) wr64(lds0 + PRING + wid * 128 + li * 8, (f32x2){inv[0], inv[1]}); }
        if constexpr (tn == 7 && pos == 7) lds_bar();
        if constexpr (tn == 7 && pos == 10) { const f32x2 o = rd64(lds0 + PRING + (wid ^ 4) * 128 + li * 8);
          inv[0] = 1.0f / (inv[0] + o[0]); inv[1] = 1.0f / (inv[1] + o[1]); PIN(inv[0]); PIN(inv[1]); }
        if constexpr (u == 31) putT(OIMG, IC<0>{}, Y);
        SBAR(); });
    });
  }
  putT(OIMG, IC<0>{}, IC<6>{}); putT(OIMG, IC<0>{}, IC<7>{}); putT(OIMG, IC<0>{}, IC<4>{}); putT(OIMG, IC<0>{}, IC<5>{});

  GAS u64* ctrl = (GAS u64*)(ws + grp * 256);
  u64 tag = 0;

  {
    const unsigned trW = lds0 + (4 * g4 + q4) * 128 + (((4 * wh + (p >> 1)) ^ ((2 * (g4 & 1) + (q4 >> 1)) << 1)) << 4) + (p & 1) * 8;
    pw = (unsigned)(head * PART_BYTES + wid * 2048) + (frag - lds0);
    bf16x8 af[112];
    f32x4 C[7][2][2];
    unsigned wb[2] = {0, 0};
    auto rd = [&](auto J, auto I) { constexpr int j = J.v, e = I.v >> 1, nt = I.v & 1;
      if constexpr (j == 0 && nt == 0) { if constexpr (e == 0) { tb = frag + OIMG + 2 * wl * 1024; PIN(tb); }
        qf[e][0] = rd128(tb + e * 8192); qf[e][1] = rd128(tb + e * 8192 + 1024); }
      af[16 * j + I.v] = rdtr2(wb[nt] + e * 4096, 2048); };
    auto mm = [&](auto J, auto I) { constexpr int j = J.v, n = I.v, e = n >> 1, nt = n & 1;
      sfor<2>([&](auto q) { if constexpr (n < 2) C[j][nt][q.v] = MFMA(af[16 * j + n], qf[e][q.v], f32x4{}); else C[j][nt][q.v] = MFMA(af[16 * j + n], qf[e][q.v], C[j][nt][q.v]); }); };
    auto fin = [&](auto J, auto E) { constexpr int j = J.v, q = E.v >> 2, nt = (E.v >> 1) & 1, e = 2 * (E.v & 1);
      unsigned w = pk(C[j][nt][q][e] * inv[q], C[j][nt][q][e + 1] * inv[q]); PIN(w); ow[q][E.v & 3] = w; };
    sfor<7>([&](auto J) { constexpr int j = J.v;
      wait_bar<cmin(inflight(IV_O + j, 0, OP_O, j), j == 0 ? inflight(IV_O, 0, OP_EP, 0) : 63)>();
      STAMP(35 + j);
      if constexpr (j == 0) {
        PIN(word);
        const u64 e = ((u64)(unsigned)__builtin_amdgcn_readfirstlane((unsigned)(word >> 32)) << 32) | (unsigned)__builtin_amdgcn_readfirstlane((unsigned)word);
        tag = e * 0x9E3779B97F4A7C15ull + 0x632BE59BD9B4E019ull;
      }
      wb[0] = trW + OSLOT(j); wb[1] = wb[0] ^ 32; PIN(wb[0]); PIN(wb[1]);
      sfor<16>([&](auto K) { constexpr int k = K.v;
        if constexpr ((k & 3) == 0) PRIO(3 - (k >> 2));
        if constexpr (k < 3) { if constexpr (j > 0) mm(IC<(j > 0 ? j - 1 : 0)>{}, IC<13 + k>{}); }
        else mm(J, IC<k - 3>{});
        SBAR();
        if constexpr (k < 2) { rd(J, IC<2 * k>{}); rd(J, IC<2 * k + 1>{}); }
        else if constexpr (k < 14) rd(J, IC<k + 2>{});
        issue(IC<IV_O + j>{}, K);
        if constexpr (j > 0 && k >= 5 && k < 13) fin(IC<(j > 0 ? j - 1 : 0)>{}, IC<(k - 5) & 7>{});
        SBAR();
      });
    });
    mm(IC<6>{}, IC<13>{}); mm(IC<6>{}, IC<14>{}); mm(IC<6>{}, IC<15>{});
    sfor<8>([&](auto e) { fin(IC<6>{}, e); });
    sfor<2>([&](auto s) { __builtin_amdgcn_raw_buffer_store_b128(ow[s.v], rsP, pw + 6 * 16384 + s.v * 1024, 0, 16); });
  }
  STAMP(42);

  volatile LAS unsigned* ready = (volatile LAS unsigned*)(uintptr_t)(lds0 + OSLOT(2));
  if (wid == 7) {
    *ready = 1u << head;
    word = ld_sc1(ws + grp * 256 + 128, (unsigned)li * 8u);
    wait_bar<1>();
    if (lane == 0) __hip_atomic_store(ctrl + 16 + head, tag, RLX_AGENT);
    wait_vm<1>(); PIN(word);
    unsigned done = ((unsigned)__ballot(word == tag) & 0xffffu) | (1u << head);
    *ready = done;
#pragma unroll 1
    for (unsigned spins = 0; spins < (1u << 18) && done != 0xffffu; ++spins) {
      done = ((unsigned)__ballot(__hip_atomic_load(ctrl + 16 + li, RLX_AGENT) == tag) & 0xffffu) | (1u << head);
      *ready = done;
      __builtin_amdgcn_s_sleep(1);
    }
    if (lane == 0 && head == 0 && done == 0xffffu) __hip_atomic_store(ctrl, tag, RLX_AGENT);
  } else {
    wait_bar<0>();
    STAMP(43);
    u32x4 v[NH];
    gather16(v, rsP, (unsigned)((wid * 16 + head) * 1024) + (frag - lds0), lds0 + OSLOT(2));
    STAMP(44);
    float s[8];
    sfor<8>([&](auto i) { s[i.v] = 0.f; });
    sfor<NH>([&](auto hh) { sfor<4>([&](auto i) {
      s[2 * i.v] += __uint_as_float(v[hh.v][i.v] << 16); s[2 * i.v + 1] += __uint_as_float(v[hh.v][i.v] & 0xffff0000u); }); });
    const int q = 32 * ((head >> 1) & 3) + 16 * (head & 1) + li, n = 64 * chunk4(wid) + 32 * (head >> 3) + 16 * (g4 & 1) + 8 * (g4 >> 1);
    *(u32x4*)(out + ((size_t)(bat * SQ + q) * (2 * DM) + br * DM + n) * 2) =
        rowpair((u32x4){pk(s[0], s[1]), pk(s[2], s[3]), pk(s[4], s[5]), pk(s[6], s[7])});
    STAMP(45);
  }
  asm volatile(".Lattn_code_end:");
}

extern "C" void kernel_launch(void* const* d_in, const int* in_sizes, int n_in,
                              void* d_out, int out_size, void* d_ws, size_t ws_size,
                              hipStream_t stream) {
  attn_fused<<<256, 512, 0, stream>>>((const char*)d_in[0], (const char*)d_in[1], (const char*)d_in[2], (const char*)d_in[3], (const char*)d_in[4],
                                      (const char*)d_in[5], (const char*)d_in[6], (const char*)d_in[7], (const char*)d_in[8], (const char*)d_in[9],
                                      (char*)d_ws, (char*)d_out);
}
```

```cpp
#include <hip/hip_runtime.h>
#include <hip/hip_bf16.h>
#include <stdint.h>

typedef short    bf16x8 __attribute__((ext_vector_type(8)));
typedef short    s16x4  __attribute__((ext_vector_type(4)));
typedef float    f32x4  __attribute__((ext_vector_type(4)));
typedef float    f32x2  __attribute__((ext_vector_type(2)));
typedef __bf16   bf16x2 __attribute__((ext_vector_type(2)));
typedef unsigned u32x4  __attribute__((ext_vector_type(4)));
typedef unsigned long long u64;
#define LAS __attribute__((address_space(3)))
#define GAS __attribute__((address_space(1)))

constexpr int NB = 8, SQ = 128, DM = 448, NH = 16, SK = 512, DH = 256;
constexpr int SLOT = 32768;
constexpr int LDS_BYTES = 5 * SLOT;
constexpr int PART_BYTES = SQ * DM * 2;
constexpr int CTRL_BYTES = 16 * 256;
constexpr float SCL = 0.0625f * 1.4426950408889634f;

#define STAMP(i) do {} while (0)
#define STAMPC(i) do {} while (0)
#define NTX 0
#define NTW 2
#define NTO 1
#define PACE3 100
#define ROT4 (bat % 7)

#define DI __device__ __forceinline__
#define MFMA(a, b, c) __builtin_amdgcn_mfma_f32_16x16x32_bf16((a), (b), (c), 0, 0, 0)
#define SBAR() __builtin_amdgcn_sched_barrier(0)
#define PIN(x) asm volatile("" : "+v"(x))
#define PINS(x) asm volatile("" : "+s"(x))
#define PRIO(n) __builtin_amdgcn_s_setprio(n)
#define RLX_AGENT __ATOMIC_RELAXED, __HIP_MEMORY_SCOPE_AGENT

template <int I> struct IC { static constexpr int v = I; };
template <int I, int N, class F> DI void sfor_(F& f) { if constexpr (I < N) { f(IC<I>{}); sfor_<I + 1, N>(f); } }
template <int N, class F> DI void sfor(F f) { sfor_<0, N>(f); }

template <int N> DI void wait_bar() { static_assert(N >= 0 && N < 64, "vmcnt"); SBAR(); asm volatile("s_waitcnt vmcnt(%0) lgkmcnt(0)\n\ts_barrier" :: "i"(N) : "memory"); SBAR(); }
template <int N> DI void wait_vm()  { static_assert(N >= 0 && N < 64, "vmcnt"); asm volatile("s_waitcnt vmcnt(%0)" :: "i"(N) : "memory"); }
template <int N> DI void pace_bar(unsigned dl) {
  static_assert(N >= 0 && N < 64, "vmcnt");
  SBAR();
  asm volatile(".Lp_%=:\n\ts_memrealtime vcc\n\ts_waitcnt vmcnt(%1) lgkmcnt(0)\n\ts_sub_i32 vcc_lo, vcc_lo, %0\n\ts_cmp_lt_i32 vcc_lo, 0\n\ts_cbranch_scc1 .Lp_%=\n\ts_barrier"
               :: "s"(dl), "i"(N) : "memory", "scc", "vcc");
  SBAR();
}
DI unsigned now32() { u64 t; asm volatile("s_memrealtime %0\n\ts_waitcnt lgkmcnt(0)" : "=s"(t) :: "memory"); return (unsigned)t; }
DI void lds_bar() { SBAR(); asm volatile("s_waitcnt lgkmcnt(0)\n\ts_barrier" ::: "memory"); SBAR(); }

DI void dma(__amdgpu_buffer_rsrc_t rs, unsigned voff, unsigned soff, unsigned ldsb) {
  asm volatile("s_mov_b32 m0, %0\n\ts_nop 0\n\tbuffer_load_dwordx4 %1, %2, %3 offen lds"
               :: "s"(ldsb), "v"(voff), "s"(rs), "s"(soff) : "memory");
}
DI void dma_nt(__amdgpu_buffer_rsrc_t rs, unsigned voff, unsigned soff, unsigned ldsb) {
  asm volatile("s_mov_b32 m0, %0\n\ts_nop 0\n\tbuffer_load_dwordx4 %1, %2, %3 offen nt lds"
               :: "s"(ldsb), "v"(voff), "s"(rs), "s"(soff) : "memory");
}
DI u32x4 ld_nt(__amdgpu_buffer_rsrc_t rs, unsigned voff, unsigned soff) {
  u32x4 v; asm volatile("buffer_load_dwordx4 %0, %1, %2, %3 offen nt" : "=v"(v) : "v"(voff), "s"(rs), "s"(soff) : "memory"); return v;
}
DI u64 ld_sc1(const void* base, unsigned voff) {
  u64 v; asm volatile("global_load_dwordx2 %0, %1, %2 sc1" : "=v"(v) : "v"(voff), "s"(base) : "memory"); return v;
}
DI void gather16(u32x4 (&v)[16], __amdgpu_buffer_rsrc_t rs, unsigned off, unsigned ra) {
  static_assert(PART_BYTES == 114688, "offsets below");
  unsigned done, cnt, f, so, t;
  asm volatile(
      "s_mov_b32 %[done], 0\n\ts_mov_b32 %[cnt], 0x400000\n"
      ".Lgl_%=:\n\tds_read_b32 %[t], %[ra]\n\ts_waitcnt lgkmcnt(0)\n\tv_readfirstlane_b32 %[f], %[t]\n\t"
      "s_andn2_b32 %[f], %[f], %[done]\n\ts_cbranch_scc0 .Lgi_%=\n\t"
      "s_bitcmp1_b32 %[f], 0\n\ts_cbranch_scc0 .Lg0_%=\n\ts_mov_b32 %[so], 0\n\tbuffer_load_dwordx4 %0, %[off], %[rs], %[so] offen sc1\n.Lg0_%=:\n\t"
      "s_bitcmp1_b32 %[f], 1\n\ts_cbranch_scc0 .Lg1_%=\n\ts_mov_b32 %[so], 114688\n\tbuffer_load_dwordx4 %1, %[off], %[rs], %[so] offen sc1\n.Lg1_%=:\n\t"
      "s_bitcmp1_b32 %[f], 2\n\ts_cbranch_scc0 .Lg2_%=\n\ts_mov_b32 %[so], 229376\n\tbuffer_load_dwordx4 %2, %[off], %[rs], %[so] offen sc1\n.Lg2_%=:\n\t"
      "s_bitcmp1_b32 %[f], 3\n\ts_cbranch_scc0 .Lg3_%=\n\ts_mov_b32 %[so], 344064\n\tbuffer_load_dwordx4 %3, %[off], %[rs], %[so] offen sc1\n.Lg3_%=:\n\t"
      "s_bitcmp1_b32 %[f], 4\n\ts_cbranch_scc0 .Lg4_%=\n\ts_mov_b32 %[so], 458752\n\tbuffer_load_dwordx4 %4, %[off], %[rs], %[so] offen sc1\n.Lg4_%=:\n\t"
      "s_bitcmp1_b32 %[f], 5\n\ts_cbranch_scc0 .Lg5_%=\n\ts_mov_b32 %[so], 573440\n\tbuffer_load_dwordx4 %5, %[off], %[rs], %[so] offen sc1\n.Lg5_%=:\n\t"
      "s_bitcmp1_b32 %[f], 6\n\ts_cbranch_scc0 .Lg6_%=\n\ts_mov_b32 %[so], 688128\n\tbuffer_load_dwordx4 %6, %[off], %[rs], %[so] offen sc1\n.Lg6_%=:\n\t"
      "s_bitcmp1_b32 %[f], 7\n\ts_cbranch_scc0 .Lg7_%=\n\ts_mov_b32 %[so], 802816\n\tbuffer_load_dwordx4 %7, %[off], %[rs], %[so] offen sc1\n.Lg7_%=:\n\t"
      "s_bitcmp1_b32 %[f], 8\n\ts_cbranch_scc0 .Lg8_%=\n\ts_mov_b32 %[so], 917504\n\tbuffer_load_dwordx4 %8, %[off], %[rs], %[so] offen sc1\n.Lg8_%=:\n\t"
      "s_bitcmp1_b32 %[f], 9\n\ts_cbranch_scc0 .Lg9_%=\n\ts_mov_b32 %[so], 1032192\n\tbuffer_load_dwordx4 %9, %[off], %[rs], %[so] offen sc1\n.Lg9_%=:\n\t"
      "s_bitcmp1_b32 %[f], 10\n\ts_cbranch_scc0 .Lg10_%=\n\ts_mov_b32 %[so], 1146880\n\tbuffer_load_dwordx4 %10, %[off], %[rs], %[so] offen sc1\n.Lg10_%=:\n\t"
      "s_bitcmp1_b32 %[f], 11\n\ts_cbranch_scc0 .Lg11_%=\n\ts_mov_b32 %[so], 1261568\n\tbuffer_load_dwordx4 %11, %[off], %[rs], %[so] offen sc1\n.Lg11_%=:\n\t"
      "s_bitcmp1_b32 %[f], 12\n\ts_cbranch_scc0 .Lg12_%=\n\ts_mov_b32 %[so], 1376256\n\tbuffer_load_dwordx4 %12, %[off], %[rs], %[so] offen sc1\n.Lg12_%=:\n\t"
      "s_bitcmp1_b32 %[f], 13\n\ts_cbranch_scc0 .Lg13_%=\n\ts_mov_b32 %[so], 1490944\n\tbuffer_load_dwordx4 %13, %[off], %[rs], %[so] offen sc1\n.Lg13_%=:\n\t"
      "s_bitcmp1_b32 %[f], 14\n\ts_cbranch_scc0 .Lg14_%=\n\ts_mov_b32 %[so], 1605632\n\tbuffer_load_dwordx4 %14, %[off], %[rs], %[so] offen sc1\n.Lg14_%=:\n\t"
      "s_bitcmp1_b32 %[f], 15\n\ts_cbranch_scc0 .Lg15_%=\n\ts_mov_b32 %[so], 1720320\n\tbuffer_load_dwordx4 %15, %[off], %[rs], %[so] offen sc1\n.Lg15_%=:\n\t"
      "s_or_b32 %[done], %[done], %[f]\n\ts_cmpk_eq_u32 %[done], 0xffff\n\ts_cbranch_scc1 .Lge_%=\n"
      ".Lgi_%=:\n\ts_sleep 1\n\ts_sub_u32 %[cnt], %[cnt], 1\n\ts_cbranch_scc0 .Lgl_%=\n"
      ".Lge_%=:\n\ts_waitcnt vmcnt(0)"
      : "=&v"(v[0]), "=&v"(v[1]), "=&v"(v[2]), "=&v"(v[3]), "=&v"(v[4]), "=&v"(v[5]), "=&v"(v[6]), "=&v"(v[7]), "=&v"(v[8]), "=&v"(v[9]), "=&v"(v[10]), "=&v"(v[11]), "=&v"(v[12]), "=&v"(v[13]), "=&v"(v[14]), "=&v"(v[15]),
        [done] "=&s"(done), [cnt] "=&s"(cnt), [f] "=&s"(f), [so] "=&s"(so), [t] "=&v"(t)
      : [ra] "v"(ra), [off] "v"(off), [rs] "s"(rs) : "memory", "scc");
}
DI unsigned pk(float a, float b) {
  const bf16x2 v = __builtin_convertvector((f32x2){a, b}, bf16x2);
  return __builtin_bit_cast(unsigned, v);
}
DI u32x4 pack2(const f32x4& a, const f32x4& b) { return (u32x4){pk(a[0], a[1]), pk(a[2], a[3]), pk(b[0], b[1]), pk(b[2], b[3])}; }
DI u32x4 rowpair(u32x4 x) {
  auto a = __builtin_amdgcn_permlane16_swap(x[0], x[2], false, false);
  auto b = __builtin_amdgcn_permlane16_swap(x[1], x[3], false, false);
  return (u32x4){a[0], b[0], a[1], b[1]};
}
DI bf16x8 rd128(unsigned a) { return *(const LAS bf16x8*)(uintptr_t)a; }
DI void wr128(unsigned a, u32x4 v) { *(LAS u32x4*)(uintptr_t)a = v; }
DI f32x2 rd64(unsigned a) { return *(const LAS f32x2*)(uintptr_t)a; }
DI void wr64(unsigned a, f32x2 v) { *(LAS f32x2*)(uintptr_t)a = v; }
DI s16x4 rdtr(unsigned a) { return __builtin_amdgcn_ds_read_tr16_b64_v4i16((LAS s16x4*)(uintptr_t)a); }
DI bf16x8 rdtr2(unsigned a, int step) {
  const s16x4 lo = rdtr(a), hi = rdtr(a + step);
  return __builtin_shufflevector(lo, hi, 0, 1, 2, 3, 4, 5, 6, 7);
}
DI float ex2(float v) { return __builtin_amdgcn_exp2f(v); }
DI float max3(float a, float b, float c) { return fmaxf(fmaxf(a, b), c); }
template <bool MAX> DI float xrows(float v) {
  auto a = __builtin_amdgcn_permlane16_swap(__float_as_uint(v), __float_as_uint(v), false, false);
  v = MAX ? fmaxf(__uint_as_float(a[0]), __uint_as_float(a[1])) : __uint_as_float(a[0]) + __uint_as_float(a[1]);
  auto b = __builtin_amdgcn_permlane32_swap(__float_as_uint(v), __float_as_uint(v), false, false);
  return MAX ? fmaxf(__uint_as_float(b[0]), __uint_as_float(b[1])) : __uint_as_float(b[0]) + __uint_as_float(b[1]);
}

constexpr int XBUF(int j) { return (j & 1) * 49152; }
constexpr int WBUF(int s) { return ((s >> 1) & 1) * 49152 + 16384 + (s & 1) * 16384; }
constexpr int KSLOT(int t) { const int m[8] = {4, 3, 4, 3, 4, 3, 0, 1}; return m[t] * SLOT; }
constexpr int VSLOT(int t) { const int m[4] = {2, 4, 3, 1}; return m[t & 3] * SLOT; }
constexpr int OSLOT(int j) { const int m[7] = {2, 4, 3, 0, 1, 2, 4}; return m[j] * SLOT; }
constexpr int QIMG = SLOT, PRING = 0, OIMG = 0;
constexpr int EORD(int n) { return (n >> 2) + 4 * ((n >> 1) & 1); }

enum { OP_X, OP_W, OP_K, OP_V, OP_O, OP_ST, OP_EXT  , OP_EP };
enum { IV_K = 15, IV_SM = 23, IV_V = 24, IV_O = 33 };
struct Op { int iv, pos, kind, idx, pc; };
struct Tab { Op op[192]; int n; };
#define QUOTA { 0, 2, 0, 2, 0, 2, 0, 2, 0, 2, 0, 3, 2, 2, 2,  3, 3, 3, 3, 3, 3, 3, 3, 2, 3, 3, 3, 3, 3, 3, 3, 4, 2, 4}
#define NEARLY 2
constexpr int NE = NEARLY, LSPAN = 96;
constexpr int NLAND = 4;
constexpr int avail(int s) {
  return s < 6 ? 0 : s == 6 ? 14 : s < 9 ? IV_K + 1 : s < 11 ? IV_K + s - 4 : s == 11 ? IV_SM : IV_V + s - 11; }
constexpr Tab make_tab() {
  Tab t{}; int n = 0;
  auto add = [&](int iv, int pos, int kind, int idx, int pc) { t.op[n] = Op{iv, pos, kind, idx, pc}; ++n; };
  for (int i = 0; i < 2; ++i) add(-1, 0, OP_X, 0, i);
  for (int i = 0; i < 2; ++i) add(-1, 0, OP_W, 0, i);
  add(-1, 0, OP_EXT, 0, 0);
  for (int i = 0; i < 2; ++i) add(-1, 1, OP_W, 1, i);
  for (int i = 0; i < 2; ++i) add(-1, 1, OP_X, 1, i);
  for (int i = 0; i < 4; ++i) add(0, 4 + i, OP_W, 2 + (i >> 1), i & 1);
  const int quota[34] = QUOTA;
  int sp = 0;
  for (int iv = 1; iv <= 33; ++iv) {
    int p0 = 2, p1 = 16;
    if (iv <= 10) {
      if ((iv & 1) == 0) { add(iv, 1, OP_X, iv / 2 + 1, 0); add(iv, 2, OP_X, iv / 2 + 1, 1); add(iv, 3, OP_W, iv + 3, 0); add(iv, 5, OP_W, iv + 3, 1); p0 = 6; }
      else { add(iv, 2, OP_W, iv + 3, 0); add(iv, 3, OP_W, iv + 3, 1); p0 = 5; }
      p1 = 8;
    } else if (iv <= 13) p1 = 8;
    else if (iv == 14 || iv == 32) p1 = 4;
    else if (iv == IV_SM) { p0 = 0; p1 = 1; }
    else if (iv == IV_V) p0 = 6;
    if (iv == IV_V + 5) add(iv, 1, OP_EP, 0, 0);
    const int q = quota[iv];
    for (int i = 0; i < q && sp < 76 && avail(sp >> 2) <= iv; ++i, ++sp) {
      const int s = sp >> 2;
      add(iv, p0 + (i * (p1 - p0)) / q, s < 8 ? OP_K : s < 16 ? OP_V : OP_O, s & 7, sp & 3);
    }
  }
  for (int j = 1; j <= 6; ++j) {
    if (j <= 2) for (int i = 0; i < 8; ++i) add(IV_O + j, i, OP_O, 1 + 2 * j + (i >> 2), i & 3);
    add(IV_O + j, 9, OP_ST, j - 1, 0); add(IV_O + j, 13, OP_ST, j - 1, 1);
  }
  t.n = n; return t;
}
constexpr Tab TAB = make_tab();
constexpr int op_first(int iv, int pos) { int i = 0; while (i < TAB.n && (TAB.op[i].iv < iv || (TAB.op[i].iv == iv && TAB.op[i].pos < pos))) ++i; return i; }
constexpr int inflight(int iv, int pos, int kind, int idx) {
  int last = -1, cnt = 0; for (int i = 0; i < TAB.n; ++i) if (TAB.op[i].kind == kind && TAB.op[i].idx == idx) { last = i; ++cnt; }
  const int lim = op_first(iv, pos);
  return (cnt != ((kind == OP_X || kind == OP_W || kind == OP_ST) ? 2 : kind >= OP_EXT ? 1 : 4) || last >= lim) ? -1 : lim - 1 - last; }
constexpr int cmin(int a, int b) { return a < b ? a : b; }

__global__ __launch_bounds__(512, 2)
void attn_fused(const char* __restrict__ xA, const char* __restrict__ kA, const char* __restrict__ vA,
                const char* __restrict__ wqA, const char* __restrict__ woA,
                const char* __restrict__ xB, const char* __restrict__ kB, const char* __restrict__ vB,
                const char* __restrict__ wqB, const char* __restrict__ woB, char* ws, char* out) {
  __shared__ __attribute__((aligned(1024))) char smem[LDS_BYTES];
  const unsigned lds0 = (unsigned)(uintptr_t)(LAS char*)smem;

  const int xcd = blockIdx.x & 7, idx = blockIdx.x >> 3;
  const int br = xcd >> 2, head = (xcd & 3) * 4 + (idx & 3), bat = idx >> 2, grp = br * NB + bat;
  const int tid = threadIdx.x, lane = tid & 63;
  const int wid = __builtin_amdgcn_readfirstlane(tid >> 6);
  const int r = lane & 31, h = lane >> 5, li = lane & 15, g4 = lane >> 4, q4 = (lane >> 2) & 3, p = lane & 3;
  const int wl = wid & 3, wh = wid >> 2;
  unsigned stv = 0; (void)stv; STAMP(0); STAMPC(62);

  const char* xg  = (br ? xB : xA) + (size_t)bat * SQ * DM * 2;
  const char* kg  = (br ? kB : kA) + (size_t)(bat * NH + head) * SK * DH * 2;
  const char* vg  = (br ? vB : vA) + (size_t)(bat * NH + head) * SK * DH * 2;
  const char* wqg = (br ? wqB : wqA) + (size_t)head * DH * 2;
  const char* wog = (br ? woB : woA) + (size_t)head * DH * DM * 2;
  const __amdgpu_buffer_rsrc_t rsX  = __builtin_amdgcn_make_buffer_rsrc((void*)xg, 0, SQ * DM * 2, 0x00020000);
  const __amdgpu_buffer_rsrc_t rsK  = __builtin_amdgcn_make_buffer_rsrc((void*)kg, 0, SK * DH * 2, 0x00020000);
  const __amdgpu_buffer_rsrc_t rsV  = __builtin_amdgcn_make_buffer_rsrc((void*)vg, 0, SK * DH * 2, 0x00020000);
  const __amdgpu_buffer_rsrc_t rsWq = __builtin_amdgcn_make_buffer_rsrc((void*)wqg, 0, (DM - 1) * NH * DH * 2 + DH * 2, 0x00020000);
  const __amdgpu_buffer_rsrc_t rsWo = __builtin_amdgcn_make_buffer_rsrc((void*)wog, 0, DH * DM * 2, 0x00020000);
  const __amdgpu_buffer_rsrc_t rsP  = __builtin_amdgcn_make_buffer_rsrc((void*)(ws + CTRL_BYTES + (size_t)grp * NH * PART_BYTES), 0, NH * PART_BYTES, 0x00020000);

  const unsigned voWq = (unsigned)h * (NH * DH * 2) + (unsigned)((r ^ ((2 * (wid & 1) + h + 4 * wh) << 1)) << 4);
  const unsigned voV  = (unsigned)h * 512u + (unsigned)((r ^ ((2 * wl + h) << 1)) << 4);
  const unsigned voK  = (unsigned)h * 512u + (unsigned)((r ^ h ^ ((2 * wid) & 15)) << 4);
  const unsigned voX  = (unsigned)(lane >> 3) * (DM * 2) + (unsigned)(((lane & 7) ^ (4 * (wid & 1) + g4)) << 4);
  unsigned voWo = 0;

  const int rot6 = (bat + 2 * (head & 3)) % 6, rot4 = ROT4;
  auto chunk1 = [&](int j) { const int c = j + rot6; return j == 0 ? 0 : c >= 7 ? c - 6 : c; };
  auto chunk4 = [&](int j) { const int c = j + rot4; return c >= 7 ? c - 7 : c; };
  const unsigned ldw = lds0 + wid * 1024;
  const unsigned frag = lds0 + lane * 16;
  unsigned so = 0;
  u32x4 kland[NLAND * 4];
  u32x4 ow[2];
  unsigned pw = 0;
  u64 word = 0;

  auto issue = [&](auto IV, auto POS) {
    constexpr int f = op_first(IV.v, POS.v), c = op_first(IV.v, POS.v + 1) - f;
    sfor<c>([&](auto I) { constexpr Op o = TAB.op[f + I.v];
      if constexpr (o.kind == OP_X) {
        if constexpr (o.pc == 0) { so = (unsigned)(wid * 8 * DM * 2 + chunk1(o.idx) * 128); PINS(so); }
        if constexpr (o.idx < NTX) dma_nt(rsX, voX, so + o.pc * (64 * DM * 2), ldw + XBUF(o.idx) + o.pc * 8192);
        else dma(rsX, voX, so + o.pc * (64 * DM * 2), ldw + XBUF(o.idx) + o.pc * 8192);
      } else if constexpr (o.kind == OP_W) {
        if constexpr (o.pc == 0) { so = (unsigned)((chunk1(o.idx >> 1) * 64 + (o.idx & 1) * 32 + 2 * wid) * (NH * DH * 2)); PINS(so); }
        if constexpr ((o.idx >> 1) < NTW) dma_nt(rsWq, voWq, so + o.pc * 16 * (NH * DH * 2), ldw + WBUF(o.idx) + o.pc * 8192);
        else dma(rsWq, voWq, so + o.pc * 16 * (NH * DH * 2), ldw + WBUF(o.idx) + o.pc * 8192);
      } else if constexpr (o.kind == OP_K) {
        if constexpr (o.idx >= 2 && o.idx < 2 + NLAND) kland[(o.idx - 2) * 4 + o.pc] = ld_nt(rsK, voK, (unsigned)(wid * 1024 + o.idx * SLOT + o.pc * 8192));
        else dma_nt(rsK, voK, (unsigned)(wid * 1024 + o.idx * SLOT + o.pc * 8192), ldw + KSLOT(o.idx) + o.pc * 8192);
      } else if constexpr (o.kind == OP_V) {
        dma_nt(rsV, voV, (unsigned)(wid * 1024 + o.idx * SLOT + o.pc * 8192), ldw + VSLOT(o.idx) + o.pc * 8192);
      } else if constexpr (o.kind == OP_O) {
        if constexpr (o.pc == 0) { so = (unsigned)(wid * 8 * DM * 2 + chunk4(o.idx) * 128); PINS(so); }
        if constexpr (o.idx < NTO) dma_nt(rsWo, voWo, so + o.pc * (64 * DM * 2), ldw + OSLOT(o.idx) + o.pc * 8192);
        else dma(rsWo, voWo, so + o.pc * (64 * DM * 2), ldw + OSLOT(o.idx) + o.pc * 8192);
      } else if constexpr (o.kind == OP_EP) {
        word = ld_sc1(ws + grp * 256, 0u);
      } else if constexpr (o.kind == OP_ST) {
        __builtin_amdgcn_raw_buffer_store_b128(ow[o.pc], rsP, pw + o.idx * 16384 + o.pc * 1024, 0, 16  );
      } }); };
  auto park = [&](auto T, auto I) { wr128(frag + wid * 1024 + KSLOT(T.v) + I.v * 8192, kland[(T.v - 2) * 4 + I.v]); };

  issue(IC<-1>{}, IC<0>{});
  {
    u64 pcv; unsigned len;
    asm volatile("s_getpc_b64 %0\n\t.Lattn_code_begin:\n\ts_mov_b32 %1, .Lattn_code_end - .Lattn_code_begin" : "=s"(pcv), "=s"(len));
    const __amdgpu_buffer_rsrc_t rsC = __builtin_amdgcn_make_buffer_rsrc((void*)(pcv & ~127ull), 0, len & ~15u, 0x00020000);
    dma(rsC, frag - lds0, (unsigned)((wid * 32 + idx) * 1024), ldw + KSLOT(0));
  }
  lds_bar();
  issue(IC<-1>{}, IC<1>{});

  const unsigned trC = (unsigned)((((8 * wl + (p >> 1)) ^ ((q4 + 4 * (g4 & 1)) << 1)) << 4) + (p & 1) * 8);

  f32x4 acc[4][4];
  bf16x8 fa[17][4], fb[17][4];
  auto mm44 = [&](auto ST, auto Z, auto H) { constexpr int st = ST.v, z = Z.v, dt = (z & 1) + 2 * (z >> 2), qt = ((z >= 2 && z < 6) ? 2 : 0) + H.v;
    if constexpr (st == 0) acc[dt][qt] = MFMA(fa[st][dt], fb[st][qt], f32x4{}); else acc[dt][qt] = MFMA(fa[st][dt], fb[st][qt], acc[dt][qt]);
    SBAR(); };
  auto rd44 = [&](auto ST, auto Z, auto LAST, auto& rdA, auto& rdB) { constexpr int st = ST.v, z = Z.v;
    if constexpr (z < 2) rdA(ST, IC<2 + z>{});
    else if constexpr (z >= 4 && st < LAST.v) {
      if constexpr (z == 4) { rdA(IC<st + 1>{}, IC<0>{}); rdB(IC<st + 1>{}, IC<0>{}); }
      if constexpr (z == 5) { rdB(IC<st + 1>{}, IC<1>{}); rdA(IC<st + 1>{}, IC<1>{}); }
      if constexpr (z >= 6) rdB(IC<st + 1>{}, IC<z - 4>{});
    } };
  unsigned tb = 0;
  float inv[4];
  auto putT = [&](int base, auto SWAP, auto I) { constexpr int e2 = I.v >> 2, qt = I.v & 3;
    if constexpr (I.v == 0) { tb = frag + base + (16 * wl + 4 * wh) * 1024; PIN(tb); }
    if constexpr (SWAP.v) wr128(tb + (8 * e2 + qt) * 1024, rowpair(pack2(acc[2 * e2][qt], acc[2 * e2 + 1][qt])));
    else wr128(tb + (8 * e2 + qt) * 1024, pack2(acc[2 * e2][qt] * inv[qt], acc[2 * e2 + 1][qt] * inv[qt])); };

  {
    const unsigned wrd = lds0 + (8 * g4 + q4) * 512 + trC;
    const unsigned xrd = lds0 + (64 * wh + li) * 128 + ((g4 ^ (li >> 1)) << 4);
    auto rdA = [&](auto S, auto D) { fa[S.v][D.v] = rdtr2((wrd ^ (D.v << 5)) + WBUF(S.v), 2048); };
    auto rdB = [&](auto S, auto Q) { fb[S.v][Q.v] = rd128((xrd ^ ((S.v & 1) << 6)) + XBUF(S.v >> 1) + Q.v * 2048); };
    wait_bar<cmin(inflight(0, 0, OP_X, 0), inflight(0, 0, OP_W, 0))>(); STAMP(1);
    sfor<4>([&](auto z) { rd44(IC<-1>{}, IC<4 + z.v>{}, IC<13>{}, rdA, rdB); }); SBAR();
    sfor<28>([&](auto U) { constexpr int u = U.v;
      constexpr int sn = (u + 1) >> 1, k0 = ((u + 1) & 1) * 4;
      if constexpr ((u & 1) == 1) {
        if constexpr (sn < 14) wait_bar<cmin(inflight(sn, 0, OP_X, sn >> 1), inflight(sn, 0, OP_W, sn < 14 ? sn : 0))>();
        else lds_bar();
        STAMP(1 + sn);
      }
      sfor<4>([&](auto Y) { constexpr int y = Y.v;
        if constexpr ((y & 1) == 0) PRIO(3 - (k0 + y) / 2);
        mm44(IC<(u >> 1)>{}, IC<(u & 1) * 4 + y>{}, IC<0>{});
        rd44(IC<(u >> 1)>{}, IC<(u & 1) * 4 + y>{}, IC<13>{}, rdA, rdB); SBAR();
        mm44(IC<(u >> 1)>{}, IC<(u & 1) * 4 + y>{}, IC<1>{});
        issue(IC<sn>{}, IC<k0 + y>{});
        if constexpr (u == 27) putT(QIMG, IC<1>{}, Y);
        SBAR(); });
    });
  }
  putT(QIMG, IC<1>{}, IC<6>{}); putT(QIMG, IC<1>{}, IC<7>{}); putT(QIMG, IC<1>{}, IC<4>{}); putT(QIMG, IC<1>{}, IC<5>{});

  bf16x8 qf[8][2];
  f32x4 S[8][2][2];
  float mx[2] = {-__builtin_inff(), -__builtin_inff()}, mh[2], nm[2], lt[2][2];
  auto tmax = [&](auto T, auto Q) { constexpr int t = T.v, q = Q.v;
    const float a = max3(S[t][0][q][0], S[t][0][q][1], S[t][0][q][2]), b = max3(S[t][1][q][0], S[t][1][q][1], S[t][1][q][2]);
    mx[q] = max3(max3(mx[q], S[t][0][q][3], S[t][1][q][3]), a, b); PIN(mx[q]); };
  auto estep = [&](auto N, auto LO, auto HI) { constexpr int n = N.v, lo = LO.v, hi = HI.v;
    if constexpr (n < hi) { constexpr int t = (n >> 4) & 7, q = (n >> 3) & 1, kt = (n >> 2) & 1, e = n & 3;
      float x = fmaf(S[t][kt][q][e], SCL, nm[q]); PIN(x); S[t][kt][q][e] = x; }
    if constexpr (n - 1 >= lo && n - 1 < hi) { constexpr int m = n - 1, t = (m >> 4) & 7, q = (m >> 3) & 1, kt = (m >> 2) & 1, e = m & 3;
      float x = ex2(S[t][kt][q][e]); PIN(x); S[t][kt][q][e] = x; }
    if constexpr (n - 2 >= lo && n - 2 < hi) { constexpr int m = n - 2, t = (m >> 4) & 7, q = (m >> 3) & 1, kt = (m >> 2) & 1, e = m & 3, a = lo > 0;
      if constexpr (m - lo < 16 && (m & 7) == 0) lt[a][q] = S[t][kt][q][e]; else { lt[a][q] += S[t][kt][q][e]; PIN(lt[a][q]); } } };
  {
    const unsigned krd = lds0 + (32 * wh + li) * 512 + (((2 * (g4 & 1) + (g4 >> 1)) ^ li) << 4);
    bf16x8 af[128];
    unsigned kb = 0;
    auto rd = [&](auto T, auto I) { constexpr int t = T.v, e = EORD(I.v), kt = I.v & 1;
      if constexpr (t == 0 && kt == 0) { if constexpr (I.v == 0) { tb = frag + QIMG + 2 * wl * 1024; PIN(tb); }
        qf[e][0] = rd128(tb + e * 8192); qf[e][1] = rd128(tb + e * 8192 + 1024); }
      af[16 * t + I.v] = rd128((kb ^ ((e & 3) << 6)) + kt * 8192 + (e >> 2) * 256); };
    auto mm = [&](auto T, auto I, auto q) { constexpr int t = T.v, n = I.v, e = EORD(n), kt = n & 1;
      if constexpr (n < 2) S[t][kt][q.v] = MFMA(af[16 * t + n], qf[e][q.v], f32x4{}); else S[t][kt][q.v] = MFMA(af[16 * t + n], qf[e][q.v], S[t][kt][q.v]); };
    sfor<8>([&](auto T) { constexpr int t = T.v;
      wait_bar<cmin(63, cmin(inflight(IV_K + t, 0, OP_K, t), inflight(IV_K + t, 0, OP_K, (t >= 1 && t <= NLAND) ? t + 1 : t)))>(); STAMP(16 + t);
      kb = krd + KSLOT(t); PIN(kb);
      sfor<16>([&](auto K) { constexpr int k = K.v;
        if constexpr ((k & 3) == 0) PRIO(3 - (k >> 2));
        auto half = [&](auto q) {
          if constexpr (k < 3) { if constexpr (t > 0) mm(IC<(t > 0 ? t - 1 : 0)>{}, IC<13 + k>{}, q); }
          else mm(T, IC<k - 3>{}, q);
          SBAR(); };
        half(IC<0>{});
        if constexpr (k < 2) { rd(T, IC<2 * k>{}); rd(T, IC<2 * k + 1>{}); }
        else if constexpr (k < 14) rd(T, IC<k + 2>{});
        issue(IC<IV_K + t>{}, K);
        if constexpr (t >= 1 && t <= NLAND && k >= 3 && k <= 12 && k % 3 == 0) park(IC<(t >= 1 && t <= NLAND) ? t + 1 : 2>{}, IC<(k / 3 - 1) & 3>{});
        SBAR();
        half(IC<1>{});
        if constexpr (t > 0 && (k == 4 || k == 5)) tmax(IC<(t > 0 ? t - 1 : 0)>{}, IC<k & 1>{});
        if constexpr (t == NE && k == 6) { sfor<2>([&](auto q) { mh[q.v] = mx[q.v] * SCL; nm[q.v] = -mh[q.v]; }); }
        if constexpr ((t > NE || (t == NE && k >= 6)) && k != 4 && k != 5) {
          constexpr int y = (t - NE) * 14 + (k < 4 ? k : k - 2) - 4, span = (8 - NE) * 14 - 4, tot = NE * 16 + 2, n0 = y * tot / span, n1 = (y + 1) * tot / span;
          sfor<n1 - n0>([&](auto i) { estep(IC<n0 + i.v>{}, IC<0>{}, IC<NE * 16>{}); }); }
        SBAR();
      });
    });
    sfor<6>([&](auto i) { mm(IC<7>{}, IC<13 + (i.v >> 1)>{}, IC<i.v & 1>{}); });
    STAMP(24);
  }
  tmax(IC<7>{}, IC<0>{}); tmax(IC<7>{}, IC<1>{});
  float fs[2];
  {
    const f32x2 mw = {xrows<true>(mx[0] * SCL), xrows<true>(mx[1] * SCL)};
    if (g4 == 0) wr64(lds0 + PRING + ((1 - wh) * 8 + 2 * wl) * 1024 + li * 8, mw);
    lds_bar(); STAMP(25);
    const f32x2 o = rd64(lds0 + PRING + (wh * 8 + 2 * wl) * 1024 + li * 8);
    sfor<2>([&](auto q) { nm[q.v] = -fmaxf(mw[q.v], o[q.v]); fs[q.v] = ex2(mh[q.v] + nm[q.v]); });
  }
  u32x4 pv;
  unsigned pwr = frag + PRING + (8 * wh + 2 * wl) * 1024, prd = frag + PRING + 4 * wh * 1024; PIN(pwr); PIN(prd);
  auto putP = [&](auto T, auto Cq) { constexpr int t = T.v, q = Cq.v >> 1, kt = Cq.v & 1;
    f32x4 x = S[t][kt][q];
    if constexpr (t < NE) x *= fs[q];
    pv[2 * kt] = pk(x[0], x[1]); pv[2 * kt + 1] = pk(x[2], x[3]);
    if constexpr (kt) wr128(pwr + (t & 1) * 16384 + q * 1024, pv); else PIN(pv); };
  sfor<4>([&](auto c) { putP(IC<0>{}, c); });
  issue(IC<IV_SM>{}, IC<0>{});
  voWo = (unsigned)(lane >> 3) * (DM * 2) + (unsigned)(((lane & 7) ^ (g4 << 1)) << 4);

  {
    const unsigned vrd = lds0 + (4 * g4 + q4) * 512 + trC;
    unsigned vb = 0;
    auto rdA = [&](auto S_, auto D) { fa[S_.v][D.v] = rdtr2((vb ^ (D.v << 5)) + (S_.v & 1) * 16384, 8192); };
    auto rdB = [&](auto S_, auto Q) { fb[S_.v][Q.v] = rd128(prd + ((S_.v >> 1) & 1) * 16384 + (S_.v & 1) * 8192 + Q.v * 1024); };
    const unsigned t3 = now32();
    wait_bar<inflight(IV_V, 0, OP_V, 0)>(); STAMP(26);
    vb = vrd + VSLOT(0); PIN(vb);
    sfor<4>([&](auto z) { rd44(IC<-1>{}, IC<4 + z.v>{}, IC<15>{}, rdA, rdB); }); SBAR();
    sfor<32>([&](auto U) { constexpr int u = U.v;
      constexpr int tn = (u + 1) >> 2, gi = (u + 1) & 3;
      if constexpr (gi == 0) {
        if constexpr (tn < 8) { pace_bar<inflight(IV_V + tn, 0, OP_V, tn < 8 ? tn : 0)>(t3 + tn * PACE3 - PACE3 / 8); vb = vrd + VSLOT(tn); PIN(vb); } else lds_bar();
        STAMP(26 + tn);
      }
      PRIO(3 - gi);
      sfor<4>([&](auto Y) { constexpr int y = Y.v, pos = 4 * gi + y, ys = 4 * u + y;
        mm44(IC<(u >> 1)>{}, IC<(u & 1) * 4 + y>{}, IC<0>{});
        rd44(IC<(u >> 1)>{}, IC<(u & 1) * 4 + y>{}, IC<15>{}, rdA, rdB);
        if constexpr (tn < 7 && (pos == 6 || pos == 7 || pos == 14 || pos == 15)) putP(IC<(tn < 7 ? tn + 1 : 0)>{}, IC<(pos & 1) + ((pos >> 3) << 1)>{});
        SBAR();
        mm44(IC<(u >> 1)>{}, IC<(u & 1) * 4 + y>{}, IC<1>{});
        issue(IC<IV_V + tn>{}, IC<pos>{});
        { constexpr int tot = (8 - NE) * 16 + 2, e0 = ys < LSPAN ? ys * tot / LSPAN : tot, e1 = ys < LSPAN ? (ys + 1) * tot / LSPAN : tot;
          sfor<e1 - e0>([&](auto i) { constexpr int n = NE * 16 + e0 + i.v; static_assert(n < NE * 16 + 2 || ys < 16 * (((n - 2) >> 4) - 1) + 2, "P tile needed earlier");
            estep(IC<n>{}, IC<NE * 16>{}, IC<128>{}); }); }
        if constexpr (tn == 7 && pos == 2) { const f32x2 l2 = {xrows<false>(fmaf(lt[0][0], fs[0], lt[1][0])), xrows<false>(fmaf(lt[0][1], fs[1], lt[1][1]))};
          if (g4 == 0) wr64(lds0 + PRING + wid * 128 + li * 8, l2); }
        if constexpr (tn == 7 && pos == 7) lds_bar();
        if constexpr (tn == 7 && (pos == 10 || pos == 11)) { constexpr int c = pos & 1;
          const unsigned a = lds0 + PRING + (2 * wh + c) * 128 + li * 8;
          const f32x2 l2 = rd64(a) + rd64(a + 512);
          inv[2 * c] = __builtin_amdgcn_rcpf(l2[0]); inv[2 * c + 1] = __builtin_amdgcn_rcpf(l2[1]); PIN(inv[2 * c]); PIN(inv[2 * c + 1]); }
        if constexpr (u == 31) putT(OIMG, IC<0>{}, Y);
        SBAR(); });
    });
  }
  putT(OIMG, IC<0>{}, IC<6>{}); putT(OIMG, IC<0>{}, IC<7>{}); putT(OIMG, IC<0>{}, IC<4>{}); putT(OIMG, IC<0>{}, IC<5>{});

  GAS u64* ctrl = (GAS u64*)(ws + grp * 256);
  u64 tag = 0;

  {
    const unsigned trW = lds0 + (4 * g4 + q4) * 128 + (((2 * (wid >> 1) + (p >> 1)) ^ ((2 * (g4 & 1) + (q4 >> 1)) << 1)) << 4) + (p & 1) * 8;
    pw = (unsigned)(head * PART_BYTES + wid * 2048) + (frag - lds0);
    bf16x8 of[8][4], af[56];
    f32x4 C[7][4];
    unsigned wb = 0;
    auto rd = [&](auto J, auto I) { constexpr int j = J.v, e = I.v >> 1, c = I.v & 1;
      if constexpr (c == 0) af[8 * j + e] = rdtr2(wb + e * 4096, 2048);
      if constexpr (j == 0) { if constexpr (I.v == 0) { tb = frag + OIMG + 4 * (wid & 1) * 1024; PIN(tb); }
        of[e][2 * c] = rd128(tb + e * 8192 + 2 * c * 1024); of[e][2 * c + 1] = rd128(tb + e * 8192 + (2 * c + 1) * 1024); } };
    auto mm = [&](auto J, auto I, auto q) { constexpr int j = J.v, n = I.v, e = n >> 1, qt = 2 * (n & 1) + q.v;
      if constexpr (n < 2) C[j][qt] = MFMA(af[8 * j + e], of[e][qt], f32x4{}); else C[j][qt] = MFMA(af[8 * j + e], of[e][qt], C[j][qt]); };
    auto fin = [&](auto J, auto E) { constexpr int j = J.v, qt = E.v >> 1, e = 2 * (E.v & 1);
      unsigned w = pk(C[j][qt][e], C[j][qt][e + 1]); PIN(w); ow[E.v >> 2][E.v & 3] = w; };
    sfor<7>([&](auto J) { constexpr int j = J.v;
      wait_bar<cmin(inflight(IV_O + j, 0, OP_O, j), j == 0 ? inflight(IV_O, 0, OP_EP, 0) : 63)>();
      STAMP(35 + j);
      if constexpr (j == 0) {
        PIN(word);
        const u64 e = ((u64)(unsigned)__builtin_amdgcn_readfirstlane((unsigned)(word >> 32)) << 32) | (unsigned)__builtin_amdgcn_readfirstlane((unsigned)word);
        tag = e * 0x9E3779B97F4A7C15ull + 0x632BE59BD9B4E019ull;
      }
      wb = trW + OSLOT(j); PIN(wb);
      sfor<16>([&](auto K) { constexpr int k = K.v;
        if constexpr ((k & 3) == 0) PRIO(3 - (k >> 2));
        auto half = [&](auto q) {
          if constexpr (k < 3) { if constexpr (j > 0) mm(IC<(j > 0 ? j - 1 : 0)>{}, IC<13 + k>{}, q); }
          else mm(J, IC<k - 3>{}, q);
          SBAR(); };
        half(IC<0>{});
        if constexpr (k < 2) { rd(J, IC<2 * k>{}); rd(J, IC<2 * k + 1>{}); }
        else if constexpr (k < 14) rd(J, IC<k + 2>{});
        SBAR();
        half(IC<1>{});
        issue(IC<IV_O + j>{}, K);
        if constexpr (j > 0 && k >= 5 && k < 13) fin(IC<(j > 0 ? j - 1 : 0)>{}, IC<(k - 5) & 7>{});
        SBAR();
      });
    });
    sfor<6>([&](auto i) { mm(IC<6>{}, IC<13 + (i.v >> 1)>{}, IC<i.v & 1>{}); });
    sfor<8>([&](auto e) { fin(IC<6>{}, e); });
    sfor<2>([&](auto s) { __builtin_amdgcn_raw_buffer_store_b128(ow[s.v], rsP, pw + 6 * 16384 + s.v * 1024, 0, 16); });
  }
  STAMP(42);

  volatile LAS unsigned* ready = (volatile LAS unsigned*)(uintptr_t)(lds0 + OSLOT(2));
  if (wid == 7) {
    *ready = 1u << head;
    word = ld_sc1(ws + grp * 256 + 128, (unsigned)li * 8u);
    wait_bar<1>();
    if (lane == 0) __hip_atomic_store(ctrl + 16 + head, tag, RLX_AGENT);
    wait_vm<1>(); PIN(word);
    unsigned done = ((unsigned)__ballot(word == tag) & 0xffffu) | (1u << head);
    *ready = done;
#pragma unroll 1
    for (unsigned spins = 0; spins < (1u << 18) && done != 0xffffu; ++spins) {
      done = ((unsigned)__ballot(__hip_atomic_load(ctrl + 16 + li, RLX_AGENT) == tag) & 0xffffu) | (1u << head);
      *ready = done;
      __builtin_amdgcn_s_sleep(1);
    }
    if (lane == 0 && head == 0 && done == 0xffffu) __hip_atomic_store(ctrl, tag, RLX_AGENT);
  } else {
    wait_bar<0>();
    STAMP(43);
    u32x4 v[NH];
    gather16(v, rsP, (unsigned)((wid * 16 + head) * 1024) + (frag - lds0), lds0 + OSLOT(2));
    STAMP(44);
    float s[8];
    sfor<8>([&](auto i) { s[i.v] = 0.f; });
    sfor<NH>([&](auto hh) { sfor<4>([&](auto i) {
      s[2 * i.v] += __uint_as_float(v[hh.v][i.v] << 16); s[2 * i.v + 1] += __uint_as_float(v[hh.v][i.v] & 0xffff0000u); }); });
    const int q = 32 * (head & 3) + 16 * (g4 & 1) + li, n = 64 * chunk4(wid) + 16 * (head >> 2) + 8 * (g4 >> 1);
    *(u32x4*)(out + ((size_t)(bat * SQ + q) * (2 * DM) + br * DM + n) * 2) =
        rowpair((u32x4){pk(s[0], s[1]), pk(s[2], s[3]), pk(s[4], s[5]), pk(s[6], s[7])});
    STAMP(45);
  }
  asm volatile(".Lattn_code_end:");
}

extern "C" void kernel_launch(void* const* d_in, const int* in_sizes, int n_in,
                              void* d_out, int out_size, void* d_ws, size_t ws_size,
                              hipStream_t stream) {
  attn_fused<<<256, 512, 0, stream>>>((const char*)d_in[0], (const char*)d_in[1], (const char*)d_in[2], (const char*)d_in[3], (const char*)d_in[4],
                                      (const char*)d_in[5], (const char*)d_in[6], (const char*)d_in[7], (const char*)d_in[8], (const char*)d_in[9],
                                      (char*)d_ws, (char*)d_out);
}
```
